```python
import torch

def _softmax(x, axis=-1):
    return torch.softmax(x, dim=axis)
def _gelu(x):
    return torch.nn.functional.gelu(x, approximate="tanh")
def _silu(x):
    return torch.nn.functional.silu(x)
def _sigmoid(x):
    return torch.sigmoid(x)
def _layernorm(x, axis=-1, eps=1e-5):
    m = x.mean(dim=axis, keepdim=True)
    v = x.var(dim=axis, keepdim=True, unbiased=False)
    return (x - m) / torch.sqrt(v + eps)
def _scatter_add(tgt, idx, src, axis=0):
    return tgt.clone().index_add_(axis, idx, src)
def _segment_sum(src, seg, n):
    out = torch.zeros((n,) + src.shape[1:], dtype=src.dtype)
    return out.index_add_(0, seg, src)
def _pad(x, pw):
    flat = tuple(v for pair in reversed(pw) for v in pair)
    return torch.nn.functional.pad(x, flat)

kernel_name = 'standalone__glu_gated_swiglu__prompt'

def setup_inputs():

    _w = (lambda a: (lambda t: t.to(torch.bfloat16) if t.is_floating_point() else t)(torch.as_tensor(a)))
    B, S, D, F = 1, 2048, 1024, 3584
    return {
        "x": _w((lambda *s: torch.randn(*s, dtype=torch.bfloat16) * 0.05)(B, S, D)),
        "w1": _w((lambda *s: torch.randn(*s, dtype=torch.bfloat16) * 0.05)(D, F) * (2.0 / D) ** 0.5),
        "w3": _w((lambda *s: torch.randn(*s, dtype=torch.bfloat16) * 0.05)(D, F) * (2.0 / D) ** 0.5),
        "w2": _w((lambda *s: torch.randn(*s, dtype=torch.bfloat16) * 0.05)(F, D) * (2.0 / F) ** 0.5),
    }

def reference(x, w1, w3, w2):
    gate = torch.einsum("bsd,df->bsf", x, w1)
    up = torch.einsum("bsd,df->bsf", x, w3)
    h = _silu(gate) * up
    out = torch.einsum("bsf,fd->bsd", h, w2)
    return out
```

```cpp
#include <hip/hip_runtime.h>
#include <hip/hip_bf16.h>
typedef short bf16x8 __attribute__((ext_vector_type(8)));
typedef float f32x4 __attribute__((ext_vector_type(4)));
typedef unsigned short u16;
constexpr int M=2048, D=1024, F=3584;
__device__ inline float bf2f(u16 b){ return __uint_as_float(((unsigned)b)<<16); }
__device__ inline u16 f2bf(float f){ unsigned u=__float_as_uint(f); u += 0x7FFFu + ((u>>16)&1u); return (u16)(u>>16);}
__global__ __launch_bounds__(256) void g1(const u16* x,const u16* w1,const u16* w3,u16* h){
  int wid=threadIdx.x>>6, lane=threadIdx.x&63;
  int f0=(blockIdx.x*4+wid)*16, m0=blockIdx.y*16;
  f32x4 ag={0,0,0,0}, au={0,0,0,0};
  for(int k=0;k<D;k+=32){
    bf16x8 a=*(const bf16x8*)(x+(size_t)(m0+(lane&15))*D+k+8*(lane>>4));
    bf16x8 b1,b3;
    for(int j=0;j<8;++j){ b1[j]=w1[(size_t)(k+8*(lane>>4)+j)*F+f0+(lane&15)]; b3[j]=w3[(size_t)(k+8*(lane>>4)+j)*F+f0+(lane&15)]; }
    ag=__builtin_amdgcn_mfma_f32_16x16x32_bf16(a,b1,ag,0,0,0);
    au=__builtin_amdgcn_mfma_f32_16x16x32_bf16(a,b3,au,0,0,0);
  }
  for(int j=0;j<4;++j){
    float g=ag[j], u=au[j];
    float s=g/(1.f+__expf(-g));
    h[(size_t)(m0+(lane>>4)*4+j)*F+f0+(lane&15)]=f2bf(s*u);
  }
}
__global__ __launch_bounds__(256) void g2(const u16* h,const u16* w2,u16* out){
  int wid=threadIdx.x>>6, lane=threadIdx.x&63;
  int n0=(blockIdx.x*4+wid)*16, m0=blockIdx.y*16;
  f32x4 ac={0,0,0,0};
  for(int k=0;k<F;k+=32){
    bf16x8 a=*(const bf16x8*)(h+(size_t)(m0+(lane&15))*F+k+8*(lane>>4));
    bf16x8 b;
    for(int j=0;j<8;++j){ b[j]=w2[(size_t)(k+8*(lane>>4)+j)*D+n0+(lane&15)]; }
    ac=__builtin_amdgcn_mfma_f32_16x16x32_bf16(a,b,ac,0,0,0);
  }
  for(int j=0;j<4;++j) out[(size_t)(m0+(lane>>4)*4+j)*D+n0+(lane&15)]=f2bf(ac[j]);
}
extern "C" void kernel_launch(void* const* d_in, const int* in_sizes, int n_in, void* d_out, int out_size, void* d_ws, size_t ws_size, hipStream_t stream) {
  const u16* x=(const u16*)d_in[0]; const u16* w1=(const u16*)d_in[1]; const u16* w3=(const u16*)d_in[2]; const u16* w2=(const u16*)d_in[3];
  u16* h=(u16*)d_ws;
  g1<<<dim3(F/64,M/16),256,0,stream>>>(x,w1,w3,h);
  g2<<<dim3(D/64,M/16),256,0,stream>>>(h,w2,(u16*)d_out);
}
```

```cpp
#include <hip/hip_runtime.h>
__global__ __launch_bounds__(256, 1) __attribute__((aligned(4096))) void swiglu_kernel(const void* x, const void* w1, const void* w3, const void* w2, void* ws, void* out) {
  unsigned tid = threadIdx.x;
  asm volatile(
    "s_load_dwordx8 s[4:11], s[0:1], 0\n\t"
    "s_load_dwordx4 s[12:15], s[0:1], 32\n\t"
    "s_getpc_b64 s[80:81]\n\t"
    "PC0%=:\n\t"
    "v_and_b32 v238, 63, v0\n\t"
    "v_lshrrev_b32 v243, 6, v0\n\t"
    "v_min_u32 v234, 55, v238\n\t"
    "v_readfirstlane_b32 s68, v243\n\t"
    "v_lshlrev_b32 v234, 7, v234\n\t"
    "s_bfe_u32 s21, s2, 196611\n\t"
    "s_mul_i32 s21, s21, 7168\n\t"
    "s_min_u32 s21, s21, CODE_END%=-PC0%=-7168\n\t"
    "s_add_u32 s80, s80, s21\n\t"
    "s_addc_u32 s81, s81, 0\n\t"
    "s_cmp_lg_u32 s68, 3\n\t"
    "s_cbranch_scc1 CP%=\n\t"
    "global_load_dword v235, v234, s[80:81]\n\t"
    "CP%=:\n\t"
    "s_bfe_u32 s16, s2, 65537\n\t"
    "s_lshl_b32 s16, s16, 2\n\t"
    "s_bfe_u32 s21, s2, 131075\n\t"
    "s_or_b32 s16, s16, s21\n\t"
    "s_bfe_u32 s17, s2, 65538\n\t"
    "s_lshl_b32 s17, s17, 4\n\t"
    "s_bfe_u32 s21, s2, 196613\n\t"
    "s_lshl_b32 s21, s21, 1\n\t"
    "s_or_b32 s17, s17, s21\n\t"
    "s_lshr_b32 s21, s2, 5\n\t"
    "s_xor_b32 s21, s21, s2\n\t"
    "s_andn2_b32 s21, 1, s21\n\t"
    "s_or_b32 s17, s17, s21\n\t"
    "v_lshrrev_b32 v239, 4, v238\n\t"
    "v_bfe_u32 v241, v238, 2, 2\n\t"
    "v_and_b32 v242, 3, v238\n\t"
    "v_and_b32 v240, 15, v238\n\t"
    "s_add_u32 s70, s16, s17\n\t"
    "s_and_b32 s70, s70, 7\n\t"
    "s_lshl_b32 s21, s70, 2\n\t"
    "s_lshr_b32 s66, 16777218, s21\n\t"
    "s_and_b32 s66, s66, 15\n\t"
    "s_lshl_b32 s64, s68, 10\n\t"
    "s_mul_i32 s21, s17, 224\n\t"
    "s_andn2_b32 s21, s21, 63\n\t"
    "s_mul_i32 s22, s68, 28672\n\t"
    "s_add_u32 s21, s21, s22\n\t"
    "s_and_b32 s22, s68, 2\n\t"
    "v_mul_u32_u24 v243, 7168, v241\n\t"
    "v_xor_b32 v244, s22, v242\n\t"
    "v_lshl_add_u32 v243, v239, 6, v243\n\t"
    "v_lshl_add_u32 v243, v244, 4, v243\n\t"
    "v_add_u32 v232, s21, v243\n\t"
    "s_mul_i32 s21, s66, 458752\n\t"
    "s_mov_b32 s63, 114688\n\t"
    "v_add_u32 v229, s21, v232\n\t"
    "s_lshl_b32 s21, s16, 19\n\t"
    "s_lshl_b32 s22, s68, 14\n\t"
    "s_add_u32 s21, s21, s22\n\t"
    "s_and_b32 s22, s68, 1\n\t"
    "s_lshl_b32 s22, s22, 2\n\t"
    "v_lshrrev_b32 v243, 3, v238\n\t"
    "v_and_b32 v244, 7, v238\n\t"
    "v_or_b32 v245, s22, v239\n\t"
    "v_xor_b32 v244, v244, v245\n\t"
    "v_lshlrev_b32 v243, 11, v243\n\t"
    "v_lshl_add_u32 v243, v244, 4, v243\n\t"
    "v_add_u32 v233, s21, v243\n\t"
    "v_lshl_add_u32 v230, s66, 7, v233\n\t"
    "v_add_u32 v231, 131072, v230\n\t"
    "s_mov_b32 s60, 65536\n\t"
    "s_mov_b32 s61, 262144\n\t"
    "s_mov_b32 s62, 327680\n\t"
    "v_accvgpr_write_b32 a144, 0\n\t"
    "v_accvgpr_write_b32 a145, 0\n\t"
    "v_accvgpr_write_b32 a146, 0\n\t"
    "v_accvgpr_write_b32 a147, 0\n\t"
    "s_mov_b32 s38, 7340032\n\t"
    "s_mov_b32 s39, 0x20000\n\t"
    "s_mov_b32 s42, 7340032\n\t"
    "s_mov_b32 s43, 0x20000\n\t"
    "s_waitcnt lgkmcnt(0)\n\t"
    "s_mov_b32 s36, s6\n\t"
    "s_and_b32 s37, s7, 0xffff\n\t"
    "s_mov_b32 s40, s8\n\t"
    "s_and_b32 s41, s9, 0xffff\n\t"
    "s_add_u32 m0, s64, 0\n\t"
    "s_nop 0\n\t"
    "buffer_load_dwordx4 v229, s[36:39], 0 offen lds\n\t"
    "s_mov_b32 s44, s4\n\t"
    "s_and_b32 s45, s5, 0xffff\n\t"
    "s_add_u32 m0, s64, 8192\n\t"
    "s_nop 0\n\t"
    "buffer_load_dwordx4 v229, s[40:43], 0 offen lds\n\t"
    "s_mov_b32 s46, 4194304\n\t"
    "s_mov_b32 s47, 0x20000\n\t"
    "s_add_u32 m0, s64, 4096\n\t"
    "s_nop 0\n\t"
    "buffer_load_dwordx4 v229, s[36:39], s63 offen lds\n\t"
    "s_mov_b32 s72, 1985229058\n\t"
    "s_mov_b32 s73, 4275878282\n\t"
    "s_add_u32 m0, s64, 12288\n\t"
    "s_nop 0\n\t"
    "buffer_load_dwordx4 v229, s[40:43], s63 offen lds\n\t"
    "s_cmp_eq_u32 s70, 1\n\t"
    "s_cselect_b32 s72, 1985225008, s72\n\t"
    "s_cselect_b32 s73, 4275878552, s73\n\t"
    "s_cmp_eq_u32 s70, 2\n\t"
    "s_cselect_b32 s72, 1985160208, s72\n\t"
    "s_cselect_b32 s73, 4275809432, s73\n\t"
    "v_add_u32 v229, 229376, v229\n\t"
    "s_add_u32 m0, s64, 16384\n\t"
    "s_nop 0\n\t"
    "buffer_load_dwordx4 v229, s[36:39], 0 offen lds\n\t"
    "s_cmp_eq_u32 s70, 3\n\t"
    "s_cselect_b32 s72, 1984123408, s72\n\t"
    "s_cselect_b32 s73, 4274772632, s73\n\t"
    "s_cmp_eq_u32 s70, 4\n\t"
    "s_cselect_b32 s72, 1967534608, s72\n\t"
    "s_cselect_b32 s73, 4258183832, s73\n\t"
    "s_cmp_eq_u32 s70, 5\n\t"
    "s_cselect_b32 s72, 1702113808, s72\n\t"
    "s_cselect_b32 s73, 3992763032, s73\n\t"
    "s_cmp_eq_u32 s70, 6\n\t"
    "s_cselect_b32 s72, 1750348289, s72\n\t"
    "s_cselect_b32 s73, 4275878551, s73\n\t"
    "s_cmp_eq_u32 s70, 7\n\t"
    "s_cselect_b32 s72, 2522100240, s72\n\t"
    "s_cselect_b32 s73, 4275874695, s73\n\t"
    "s_add_u32 m0, s64, 24576\n\t"
    "s_nop 0\n\t"
    "buffer_load_dwordx4 v229, s[40:43], 0 offen lds\n\t"
    "s_mov_b64 s[74:75], s[72:73]\n\t"
    "s_and_b32 s21, s68, 1\n\t"
    "s_lshl_b32 s21, s21, 13\n\t"
    "s_add_u32 s21, s21, 65536\n\t"
    "v_mfma_f32_32x32x16_bf16 v[0:15], a[144:147], a[144:147], 0\n\t"
    "s_add_u32 m0, s64, 20480\n\t"
    "s_nop 0\n\t"
    "buffer_load_dwordx4 v229, s[36:39], s63 offen lds\n\t"
    "v_lshrrev_b32 v243, 1, v240\n\t"
    "v_xor_b32 v243, v243, v239\n\t"
    "v_lshlrev_b32 v243, 4, v243\n\t"
    "v_mfma_f32_32x32x16_bf16 v[16:31], a[144:147], a[144:147], 0\n\t"
    "v_lshl_add_u32 v243, v240, 7, v243\n\t"
    "s_add_u32 m0, s64, 28672\n\t"
    "s_nop 0\n\t"
    "buffer_load_dwordx4 v229, s[40:43], s63 offen lds\n\t"
    "v_add_u32 v224, s21, v243\n\t"
    "v_xor_b32 v225, 64, v224\n\t"
    "v_mfma_f32_32x32x16_bf16 v[32:47], a[144:147], a[144:147], 0\n\t"
    "v_and_b32 v245, 1, v239\n\t"
    "v_lshlrev_b32 v245, 5, v245\n\t"
    "s_lshr_b64 s[74:75], s[74:75], 4\n\t"
    "s_and_b32 s71, s74, 15\n\t"
    "s_mul_i32 s71, s71, 458752\n\t"
    "v_add_u32 v229, s71, v232\n\t"
    "s_add_u32 m0, s64, 65536\n\t"
    "s_nop 0\n\t"
    "buffer_load_dwordx4 v230, s[44:47], 0 offen lds\n\t"
    "v_lshl_add_u32 v245, v241, 6, v245\n\t"
    "v_mfma_f32_32x32x16_bf16 v[48:63], a[144:147], a[144:147], 0\n\t"
    "v_lshl_add_u32 v243, v239, 11, v245\n\t"
    "v_lshl_add_u32 v244, v242, 3, v243\n\t"
    "s_and_b32 s23, s17, 1\n\t"
    "s_add_u32 m0, s64, 69632\n\t"
    "s_nop 0\n\t"
    "buffer_load_dwordx4 v230, s[44:47], s60 offen lds\n\t"
    "v_mfma_f32_32x32x16_bf16 v[64:79], a[144:147], a[144:147], 0\n\t"
    "s_lshl_b32 s21, s23, 5\n\t"
    "s_lshr_b32 s22, s68, 1\n\t"
    "s_lshl_b32 s22, s22, 9\n\t"
    "v_mfma_f32_32x32x16_bf16 v[80:95], a[144:147], a[144:147], 0\n\t"
    "s_add_u32 m0, s64, 73728\n\t"
    "s_nop 0\n\t"
    "buffer_load_dwordx4 v230, s[44:47], s61 offen lds\n\t"
    "v_xor_b32 v226, s21, v244\n\t"
    "v_add_u32 v226, s22, v226\n\t"
    "s_lshl_b32 s22, s23, 8\n\t"
    "v_mfma_f32_32x32x16_bf16 v[96:111], a[144:147], a[144:147], 0\n\t"
    "v_xor_b32 v227, 32, v226\n\t"
    "s_add_u32 m0, s64, 77824\n\t"
    "s_nop 0\n\t"
    "buffer_load_dwordx4 v230, s[44:47], s62 offen lds\n\t"
    "v_add_u32 v227, s22, v227\n\t"
    "s_xor_b32 s21, s21, 32\n\t"
    "v_mfma_f32_32x32x16_bf16 v[112:127], a[144:147], a[144:147], 0\n\t"
    "v_xor_b32 v243, s21, v243\n\t"
    "s_and_b32 s21, s68, 2\n\t"
    "s_add_u32 m0, s64, 81920\n\t"
    "s_nop 0\n\t"
    "buffer_load_dwordx4 v231, s[44:47], 0 offen lds\n\t"
    "s_lshl_b32 s21, s21, 3\n\t"
    "v_mfma_f32_32x32x16_bf16 v[128:143], a[144:147], a[144:147], 0\n\t"
    "s_add_u32 s21, s21, s22\n\t"
    "s_add_u32 s21, s21, 256\n\t"
    "v_lshrrev_b32 v244, 1, v242\n\t"
    "s_add_u32 m0, s64, 86016\n\t"
    "s_nop 0\n\t"
    "buffer_load_dwordx4 v231, s[44:47], s60 offen lds\n\t"
    "v_mfma_f32_32x32x16_bf16 v[144:159], a[144:147], a[144:147], 0\n\t"
    "v_lshl_add_u32 v243, v244, 13, v243\n\t"
    "v_and_b32 v244, 1, v242\n\t"
    "v_lshl_add_u32 v243, v244, 3, v243\n\t"
    "v_mfma_f32_32x32x16_bf16 v[160:175], a[144:147], a[144:147], 0\n\t"
    "s_add_u32 m0, s64, 90112\n\t"
    "s_nop 0\n\t"
    "buffer_load_dwordx4 v231, s[44:47], s61 offen lds\n\t"
    "v_add_u32 v228, s21, v243\n\t"
    "v_lshl_add_u32 v243, v239, 10, v245\n\t"
    "v_lshl_add_u32 v243, v242, 3, v243\n\t"
    "v_mfma_f32_32x32x16_bf16 v[176:191], a[144:147], a[144:147], 0\n\t"
    "s_and_b32 s21, s68, 2\n\t"
    "s_add_u32 m0, s64, 94208\n\t"
    "s_nop 0\n\t"
    "buffer_load_dwordx4 v231, s[44:47], s62 offen lds\n\t"
    "s_lshl_b32 s21, s21, 12\n\t"
    "v_add_u32 v251, s21, v243\n\t"
    "v_mfma_f32_32x32x16_bf16 v[192:207], a[144:147], a[144:147], 0\n\t"
    "v_mfma_f32_32x32x16_bf16 v[208:223], a[144:147], a[144:147], 0\n\t"
    "s_bfe_u32 s18, s2, 65536\n\t"
    "s_lshr_b64 s[72:73], s[72:73], 4\n\t"
    "s_and_b32 s66, s72, 15\n\t"
    "v_lshl_add_u32 v230, s66, 7, v233\n\t"
    "v_add_u32 v231, 131072, v230\n\t"
    "s_bfe_u32 s19, s2, 131073\n\t"
    "s_lshl_b32 s19, s19, 2\n\t"
    "s_bfe_u32 s21, s2, 131075\n\t"
    "s_or_b32 s19, s19, s21\n\t"
    "s_bfe_u32 s20, s2, 196613\n\t"
    "s_mov_b32 s52, s12\n\t"
    "s_and_b32 s53, s13, 0xffff\n\t"
    "s_mov_b32 s54, 26214400\n\t"
    "s_mov_b32 s55, 0x20000\n\t"
    "s_lshr_b32 s21, s19, 3\n\t"
    "s_lshl_b32 s21, s21, 1\n\t"
    "s_lshl_b32 s22, s18, 2\n\t"
    "s_or_b32 s21, s21, s22\n\t"
    "s_and_b32 s22, s19, 6\n\t"
    "s_lshl_b32 s22, s22, 2\n\t"
    "s_or_b32 s21, s21, s22\n\t"
    "s_or_b32 s21, s21, 1\n\t"
    "v_lshrrev_b32 v244, 1, v240\n\t"
    "v_lshl_add_u32 v243, v244, 5, s21\n\t"
    "v_xor_b32 v244, v244, v240\n\t"
    "v_and_b32 v244, 1, v244\n\t"
    "v_xor_b32 v243, v243, v244\n\t"
    "v_and_b32 v244, 1, v239\n\t"
    "v_lshlrev_b32 v244, 8, v244\n\t"
    "v_lshl_add_u32 v243, v243, 9, v244\n\t"
    "v_add_u32 v248, 25165824, v243\n\t"
    "s_lshl_b32 s21, s2, 9\n\t"
    "s_add_u32 s21, s21, 25165824\n\t"
    "s_xor_b32 s22, s21, 512\n\t"
    "v_writelane_b32 v248, s22, 32\n\t"
    "v_writelane_b32 v248, s21, 34\n\t"
    "s_add_u32 s22, s21, 256\n\t"
    "v_writelane_b32 v248, s22, 35\n\t"
    "s_add_u32 s22, s21, 128\n\t"
    "v_writelane_b32 v248, s22, 37\n\t"
    "s_lshl_b32 s21, s2, 15\n\t"
    "s_lshl_b32 s22, s68, 13\n\t"
    "s_add_u32 s21, s21, s22\n\t"
    "s_add_u32 s21, s21, 16777216\n\t"
    "s_add_u32 m0, s64, 32768\n\t"
    "s_nop 0\n\t"
    "buffer_load_dwordx4 v229, s[36:39], 0 offen lds\n\t"
    "s_add_u32 m0, s64, 40960\n\t"
    "s_nop 0\n\t"
    "buffer_load_dwordx4 v229, s[40:43], 0 offen lds\n\t"
    "s_mov_b64 exec, 0\n\t"
    "s_cmp_eq_u32 s68, 1\n\t"
    "s_cbranch_scc1 ST2_%=\n\t"
    "s_cmp_eq_u32 s68, 2\n\t"
    "s_cbranch_scc1 ST3_%=\n\t"
    "s_cmp_eq_u32 s68, 3\n\t"
    "s_cbranch_scc1 ST4_%=\n\t"
    "s_branch ST1_%=\n\t"
    "STR%=:\n\t"
    "s_mov_b64 exec, -1\n\t"
    "s_add_u32 m0, s64, 36864\n\t"
    "s_nop 0\n\t"
    "buffer_load_dwordx4 v229, s[36:39], s63 offen lds\n\t"
    "s_add_u32 m0, s64, 45056\n\t"
    "s_nop 0\n\t"
    "buffer_load_dwordx4 v229, s[40:43], s63 offen lds\n\t"
    "v_add_u32 v229, 229376, v229\n\t"
    "s_add_u32 m0, s64, 98304\n\t"
    "s_nop 0\n\t"
    "buffer_load_dwordx4 v230, s[44:47], 0 offen lds\n\t"
    "s_add_u32 m0, s64, 49152\n\t"
    "s_nop 0\n\t"
    "buffer_load_dwordx4 v229, s[36:39], 0 offen lds\n\t"
    "s_waitcnt vmcnt(10)\n\t"
    "s_barrier\n\t"
    "ds_read_b128 a[112:115], v224 offset:0\n\t"
    "ds_read_b128 a[116:119], v225 offset:0\n\t"
    "ds_read_b64_tr_b16 a[0:1], v226 offset:0\n\t"
    "ds_read_b64_tr_b16 a[2:3], v226 offset:1024\n\t"
    "ds_read_b64_tr_b16 a[4:5], v226 offset:16384\n\t"
    "ds_read_b64_tr_b16 a[6:7], v226 offset:17408\n\t"
    "ds_read_b64_tr_b16 a[8:9], v227 offset:0\n\t"
    "ds_read_b64_tr_b16 a[10:11], v227 offset:1024\n\t"
    "ds_read_b64_tr_b16 a[12:13], v227 offset:16384\n\t"
    "ds_read_b64_tr_b16 a[14:15], v227 offset:17408\n\t"
    "ds_read_b64_tr_b16 a[16:17], v226 offset:256\n\t"
    "ds_read_b64_tr_b16 a[18:19], v226 offset:1280\n\t"
    "ds_read_b64_tr_b16 a[20:21], v226 offset:16640\n\t"
    "ds_read_b64_tr_b16 a[22:23], v226 offset:17664\n\t"
    "s_waitcnt lgkmcnt(10)\n\t"
    "v_mfma_f32_16x16x32_bf16 v[0:3], a[0:3], a[112:115], v[0:3]\n\t"
    "ds_read_b64_tr_b16 a[24:25], v226 offset:8192\n\t"
    "ds_read_b64_tr_b16 a[26:27], v226 offset:9216\n\t"
    "v_lshl_add_u32 v253, v238, 4, s21\n\t"
    "s_waitcnt lgkmcnt(10)\n\t"
    "v_mfma_f32_16x16x32_bf16 v[0:3], a[4:7], a[116:119], v[0:3]\n\t"
    "ds_read_b64_tr_b16 a[28:29], v226 offset:24576\n\t"
    "ds_read_b64_tr_b16 a[30:31], v226 offset:25600\n\t"
    "s_add_u32 s70, s68, 1\n\t"
    "s_waitcnt lgkmcnt(10)\n\t"
    "v_mfma_f32_16x16x32_bf16 v[4:7], a[8:11], a[112:115], v[4:7]\n\t"
    "ds_read_b64_tr_b16 a[32:33], v227 offset:8192\n\t"
    "ds_read_b64_tr_b16 a[34:35], v227 offset:9216\n\t"
    "s_lshl_b32 s21, s68, 8\n\t"
    "s_waitcnt lgkmcnt(10)\n\t"
    "v_mfma_f32_16x16x32_bf16 v[4:7], a[12:15], a[116:119], v[4:7]\n\t"
    "ds_read_b64_tr_b16 a[36:37], v227 offset:24576\n\t"
    "ds_read_b64_tr_b16 a[38:39], v227 offset:25600\n\t"
    "s_add_u32 s21, s21, s2\n\t"
    "s_waitcnt lgkmcnt(10)\n\t"
    "v_mfma_f32_16x16x32_bf16 v[8:11], a[16:19], a[112:115], v[8:11]\n\t"
    "ds_read_b64_tr_b16 a[40:41], v226 offset:8448\n\t"
    "ds_read_b64_tr_b16 a[42:43], v226 offset:9472\n\t"
    "s_add_u32 m0, s64, 102400\n\t"
    "s_mul_i32 s21, s21, 7168\n\t"
    "s_nop 0\n\t"
    "buffer_load_dwordx4 v230, s[44:47], s60 offen lds\n\t"
    "s_waitcnt lgkmcnt(10)\n\t"
    "v_mfma_f32_16x16x32_bf16 v[8:11], a[20:23], a[116:119], v[8:11]\n\t"
    "ds_read_b64_tr_b16 a[44:45], v226 offset:24832\n\t"
    "ds_read_b64_tr_b16 a[46:47], v226 offset:25856\n\t"
    "s_add_u32 s76, s10, s21\n\t"
    "s_addc_u32 s77, s11, 0\n\t"
    "s_waitcnt lgkmcnt(10)\n\t"
    "v_mfma_f32_16x16x32_bf16 v[12:15], a[24:27], a[112:115], v[12:15]\n\t"
    "ds_read_b64_tr_b16 a[48:49], v228 offset:0\n\t"
    "ds_read_b64_tr_b16 a[50:51], v228 offset:1024\n\t"
    "s_mov_b32 s65, 0\n\t"
    "s_waitcnt lgkmcnt(10)\n\t"
    "v_mfma_f32_16x16x32_bf16 v[12:15], a[28:31], a[116:119], v[12:15]\n\t"
    "ds_read_b64_tr_b16 a[52:53], v228 offset:16384\n\t"
    "ds_read_b64_tr_b16 a[54:55], v228 offset:17408\n\t"
    "s_waitcnt lgkmcnt(10)\n\t"
    "v_mfma_f32_16x16x32_bf16 v[16:19], a[32:35], a[112:115], v[16:19]\n\t"
    "ds_read_b128 a[120:123], v224 offset:2048\n\t"
    "ds_read_b128 a[124:127], v225 offset:2048\n\t"
    "s_add_u32 m0, s64, 57344\n\t"
    "s_nop 0\n\t"
    "buffer_load_dwordx4 v229, s[40:43], 0 offen lds\n\t"
    "s_waitcnt lgkmcnt(10)\n\t"
    "v_mfma_f32_16x16x32_bf16 v[16:19], a[36:39], a[116:119], v[16:19]\n\t"
    "ds_read_b128 a[128:131], v224 offset:4096\n\t"
    "ds_read_b128 a[132:135], v225 offset:4096\n\t"
    "s_waitcnt lgkmcnt(10)\n\t"
    "v_mfma_f32_16x16x32_bf16 v[20:23], a[40:43], a[112:115], v[20:23]\n\t"
    "ds_read_b128 a[136:139], v224 offset:6144\n\t"
    "ds_read_b128 a[140:143], v225 offset:6144\n\t"
    "s_waitcnt lgkmcnt(10)\n\t"
    "v_mfma_f32_16x16x32_bf16 v[20:23], a[44:47], a[116:119], v[20:23]\n\t"
    "s_waitcnt lgkmcnt(8)\n\t"
    "v_mfma_f32_16x16x32_bf16 v[24:27], a[48:51], a[112:115], v[24:27]\n\t"
    "s_add_u32 m0, s64, 106496\n\t"
    "s_nop 0\n\t"
    "buffer_load_dwordx4 v230, s[44:47], s61 offen lds\n\t"
    "s_waitcnt lgkmcnt(6)\n\t"
    "v_mfma_f32_16x16x32_bf16 v[24:27], a[52:55], a[116:119], v[24:27]\n\t"
    "s_waitcnt lgkmcnt(4)\n\t"
    "v_mfma_f32_16x16x32_bf16 v[28:31], a[0:3], a[120:123], v[28:31]\n\t"
    "v_mfma_f32_16x16x32_bf16 v[28:31], a[4:7], a[124:127], v[28:31]\n\t"
    "v_mfma_f32_16x16x32_bf16 v[32:35], a[8:11], a[120:123], v[32:35]\n\t"
    "s_add_u32 m0, s64, 53248\n\t"
    "s_nop 0\n\t"
    "buffer_load_dwordx4 v229, s[36:39], s63 offen lds\n\t"
    "v_mfma_f32_16x16x32_bf16 v[32:35], a[12:15], a[124:127], v[32:35]\n\t"
    "v_mfma_f32_16x16x32_bf16 v[36:39], a[16:19], a[120:123], v[36:39]\n\t"
    "v_mfma_f32_16x16x32_bf16 v[36:39], a[20:23], a[124:127], v[36:39]\n\t"
    "v_mfma_f32_16x16x32_bf16 v[40:43], a[24:27], a[120:123], v[40:43]\n\t"
    "s_add_u32 m0, s64, 110592\n\t"
    "s_nop 0\n\t"
    "buffer_load_dwordx4 v230, s[44:47], s62 offen lds\n\t"
    "v_mfma_f32_16x16x32_bf16 v[40:43], a[28:31], a[124:127], v[40:43]\n\t"
    "v_mfma_f32_16x16x32_bf16 v[44:47], a[32:35], a[120:123], v[44:47]\n\t"
    "v_mfma_f32_16x16x32_bf16 v[44:47], a[36:39], a[124:127], v[44:47]\n\t"
    "v_mfma_f32_16x16x32_bf16 v[48:51], a[40:43], a[120:123], v[48:51]\n\t"
    "s_add_u32 m0, s64, 61440\n\t"
    "s_nop 0\n\t"
    "buffer_load_dwordx4 v229, s[40:43], s63 offen lds\n\t"
    "v_mfma_f32_16x16x32_bf16 v[48:51], a[44:47], a[124:127], v[48:51]\n\t"
    "v_mfma_f32_16x16x32_bf16 v[52:55], a[48:51], a[120:123], v[52:55]\n\t"
    "v_mfma_f32_16x16x32_bf16 v[52:55], a[52:55], a[124:127], v[52:55]\n\t"
    "s_lshr_b64 s[72:73], s[72:73], 4\n\t"
    "s_and_b32 s66, s72, 15\n\t"
    "v_lshl_add_u32 v230, s66, 7, v233\n\t"
    "s_lshr_b64 s[74:75], s[74:75], 4\n\t"
    "s_and_b32 s71, s74, 15\n\t"
    "s_mul_i32 s71, s71, 458752\n\t"
    "v_add_u32 v229, s71, v232\n\t"
    "v_accvgpr_write_b32 a148, -1\n\t"
    "v_accvgpr_write_b32 a149, -1\n\t"
    "v_accvgpr_write_b32 a150, -1\n\t"
    "v_accvgpr_write_b32 a151, -1\n\t"
    "s_waitcnt vmcnt(8)\n\t"
    "s_branch LA2%=\n\t"
    "PFO%=:\n\t"
    "global_load_dword v235, v234, s[76:77]\n\t"
    "s_branch PFS%=\n\t"
    "SMO%=:\n\t"
    "s_cmp_eq_u32 s68, 0\n\t"
    "s_cselect_b32 exec_lo, -1, 0xffff\n\t"
    "s_cselect_b32 exec_hi, 0x2d, 0\n\t"
    "buffer_load_dword v249, v248, s[52:55], 0 offen sc1\n\t"
    "s_mov_b64 exec, -1\n\t"
    "s_branch SMS%=\n\t"
    ".p2align 6\n\t"
    "LA%=:\n\t"
    "s_waitcnt lgkmcnt(0)\n\t"
    "v_mfma_f32_16x16x32_bf16 v[0:3], a[0:3], a[112:115], v[0:3]\n\t"
    "ds_read_b128 a[128:131], v224 offset:4096\n\t"
    "v_mfma_f32_16x16x32_bf16 v[0:3], a[4:7], a[116:119], v[0:3]\n\t"
    "ds_read_b128 a[132:135], v225 offset:4096\n\t"
    "v_mfma_f32_16x16x32_bf16 v[4:7], a[8:11], a[112:115], v[4:7]\n\t"
    "s_and_b32 s21, s68, 1\n\t"
    "s_lshl_b32 s22, s16, 1\n\t"
    "v_mfma_f32_16x16x32_bf16 v[4:7], a[12:15], a[116:119], v[4:7]\n\t"
    "s_add_u32 m0, s64, 53248\n\t"
    "v_mfma_f32_16x16x32_bf16 v[8:11], a[16:19], a[112:115], v[8:11]\n\t"
    "buffer_load_dwordx4 v229, s[36:39], s63 offen lds\n\t"
    "v_mfma_f32_16x16x32_bf16 v[8:11], a[20:23], a[116:119], v[8:11]\n\t"
    "s_add_u32 s21, s21, s22\n\t"
    "s_mul_i32 s21, s21, 917504\n\t"
    "v_mfma_f32_16x16x32_bf16 v[12:15], a[24:27], a[112:115], v[12:15]\n\t"
    "s_mul_i32 s22, s17, 3584\n\t"
    "s_add_u32 s21, s21, s22\n\t"
    "v_mfma_f32_16x16x32_bf16 v[12:15], a[28:31], a[116:119], v[12:15]\n\t"
    "s_lshr_b32 s23, s68, 1\n\t"
    "s_lshl_b32 s22, s23, 11\n\t"
    "v_mfma_f32_16x16x32_bf16 v[16:19], a[32:35], a[112:115], v[16:19]\n\t"
    "s_add_u32 s22, s22, s21\n\t"
    "v_mfma_f32_16x16x32_bf16 v[16:19], a[36:39], a[116:119], v[16:19]\n\t"
    "s_add_u32 m0, s64, 110592\n\t"
    "v_mfma_f32_16x16x32_bf16 v[20:23], a[40:43], a[112:115], v[20:23]\n\t"
    "buffer_load_dwordx4 v230, s[44:47], s62 offen lds\n\t"
    "v_mfma_f32_16x16x32_bf16 v[20:23], a[44:47], a[116:119], v[20:23]\n\t"
    "v_and_b32 v243, 1, v239\n\t"
    "v_mfma_f32_16x16x32_bf16 v[24:27], a[48:51], a[112:115], v[24:27]\n\t"
    "v_lshrrev_b32 v244, 1, v239\n\t"
    "v_mfma_f32_16x16x32_bf16 v[24:27], a[52:55], a[116:119], v[24:27]\n\t"
    "v_mfma_f32_16x16x32_bf16 v[28:31], a[0:3], a[120:123], v[28:31]\n\t"
    "ds_read_b128 a[136:139], v224 offset:6144\n\t"
    "v_mfma_f32_16x16x32_bf16 v[28:31], a[4:7], a[124:127], v[28:31]\n\t"
    "ds_read_b128 a[140:143], v225 offset:6144\n\t"
    "v_mfma_f32_16x16x32_bf16 v[32:35], a[8:11], a[120:123], v[32:35]\n\t"
    "s_add_u32 m0, s64, 61440\n\t"
    "v_mfma_f32_16x16x32_bf16 v[32:35], a[12:15], a[124:127], v[32:35]\n\t"
    "buffer_load_dwordx4 v229, s[40:43], s63 offen lds\n\t"
    "v_mfma_f32_16x16x32_bf16 v[36:39], a[16:19], a[120:123], v[36:39]\n\t"
    "v_mul_u32_u24 v245, 114688, v243\n\t"
    "v_mfma_f32_16x16x32_bf16 v[36:39], a[20:23], a[124:127], v[36:39]\n\t"
    "s_lshr_b64 s[72:73], s[72:73], 4\n\t"
    "s_and_b32 s66, s72, 15\n\t"
    "v_mfma_f32_16x16x32_bf16 v[40:43], a[24:27], a[120:123], v[40:43]\n\t"
    "v_lshl_add_u32 v245, v244, 8, v245\n\t"
    "v_mfma_f32_16x16x32_bf16 v[40:43], a[28:31], a[124:127], v[40:43]\n\t"
    "v_lshl_add_u32 v230, s66, 7, v233\n\t"
    "v_mfma_f32_16x16x32_bf16 v[44:47], a[32:35], a[120:123], v[44:47]\n\t"
    "v_lshl_add_u32 v245, v240, 4, v245\n\t"
    "v_mfma_f32_16x16x32_bf16 v[44:47], a[36:39], a[124:127], v[44:47]\n\t"
    "s_lshr_b64 s[74:75], s[74:75], 4\n\t"
    "s_and_b32 s71, s74, 15\n\t"
    "v_mfma_f32_16x16x32_bf16 v[48:51], a[40:43], a[120:123], v[48:51]\n\t"
    "v_add_u32 v236, s22, v245\n\t"
    "v_mfma_f32_16x16x32_bf16 v[48:51], a[44:47], a[124:127], v[48:51]\n\t"
    "s_mul_i32 s71, s71, 458752\n\t"
    "v_mfma_f32_16x16x32_bf16 v[52:55], a[48:51], a[120:123], v[52:55]\n\t"
    "s_add_u32 s23, s23, 6\n\t"
    "s_lshl_b32 s23, s23, 8\n\t"
    "v_mfma_f32_16x16x32_bf16 v[52:55], a[52:55], a[124:127], v[52:55]\n\t"
    "v_add_u32 v229, s71, v232\n\t"
    "LA2%=:\n\t"
    "s_waitcnt vmcnt(9) lgkmcnt(2)\n\t"
    "s_barrier\n\t"
    "v_mfma_f32_16x16x32_bf16 v[56:59], a[0:3], a[128:131], v[56:59]\n\t"
    "ds_read_b128 a[112:115], v224 offset:16384\n\t"
    "v_mfma_f32_16x16x32_bf16 v[56:59], a[4:7], a[132:135], v[56:59]\n\t"
    "ds_read_b128 a[116:119], v225 offset:16384\n\t"
    "v_mfma_f32_16x16x32_bf16 v[60:63], a[8:11], a[128:131], v[60:63]\n\t"
    "s_add_u32 m0, s64, 114688\n\t"
    "v_mfma_f32_16x16x32_bf16 v[60:63], a[12:15], a[132:135], v[60:63]\n\t"
    "buffer_load_dwordx4 v231, s[44:47], 0 offen lds\n\t"
    "v_mfma_f32_16x16x32_bf16 v[64:67], a[16:19], a[128:131], v[64:67]\n\t"
    "ds_read_b64_tr_b16 a[56:57], v226 offset:32768\n\t"
    "v_mfma_f32_16x16x32_bf16 v[64:67], a[20:23], a[132:135], v[64:67]\n\t"
    "s_cmp_eq_u32 s65, s70\n\t"
    "s_cbranch_scc1 PFO%=\n\t"
    "PFS%=:\n\t"
    "v_mfma_f32_16x16x32_bf16 v[68:71], a[24:27], a[128:131], v[68:71]\n\t"
    "ds_read_b64_tr_b16 a[58:59], v226 offset:33792\n\t"
    "v_mfma_f32_16x16x32_bf16 v[68:71], a[28:31], a[132:135], v[68:71]\n\t"
    "s_add_u32 s21, s21, s23\n\t"
    "v_mfma_f32_16x16x32_bf16 v[72:75], a[32:35], a[128:131], v[72:75]\n\t"
    "s_add_u32 m0, s64, 0\n\t"
    "v_mfma_f32_16x16x32_bf16 v[72:75], a[36:39], a[132:135], v[72:75]\n\t"
    "buffer_load_dwordx4 v229, s[36:39], 0 offen lds\n\t"
    "v_mfma_f32_16x16x32_bf16 v[76:79], a[40:43], a[128:131], v[76:79]\n\t"
    "ds_read_b64_tr_b16 a[64:65], v227 offset:32768\n\t"
    "v_mfma_f32_16x16x32_bf16 v[76:79], a[44:47], a[132:135], v[76:79]\n\t"
    "v_lshl_add_u32 v245, v243, 1, v244\n\t"
    "v_mfma_f32_16x16x32_bf16 v[80:83], a[48:51], a[128:131], v[80:83]\n\t"
    "ds_read_b64_tr_b16 a[66:67], v227 offset:33792\n\t"
    "v_mfma_f32_16x16x32_bf16 v[80:83], a[52:55], a[132:135], v[80:83]\n\t"
    "s_waitcnt lgkmcnt(6)\n\t"
    "v_mfma_f32_16x16x32_bf16 v[84:87], a[0:3], a[136:139], v[84:87]\n\t"
    "ds_read_b128 a[120:123], v224 offset:18432\n\t"
    "v_mfma_f32_16x16x32_bf16 v[84:87], a[4:7], a[140:143], v[84:87]\n\t"
    "ds_read_b128 a[124:127], v225 offset:18432\n\t"
    "s_add_u32 m0, s64, 118784\n\t"
    "v_mfma_f32_16x16x32_bf16 v[88:91], a[8:11], a[136:139], v[88:91]\n\t"
    "buffer_load_dwordx4 v231, s[44:47], s60 offen lds\n\t"
    "v_mfma_f32_16x16x32_bf16 v[88:91], a[12:15], a[140:143], v[88:91]\n\t"
    "ds_read_b64_tr_b16 a[72:73], v226 offset:33024\n\t"
    "v_mfma_f32_16x16x32_bf16 v[92:95], a[16:19], a[136:139], v[92:95]\n\t"
    "v_mul_u32_u24 v245, 114688, v245\n\t"
    "v_mfma_f32_16x16x32_bf16 v[92:95], a[20:23], a[140:143], v[92:95]\n\t"
    "v_lshl_add_u32 v245, v240, 4, v245\n\t"
    "v_mfma_f32_16x16x32_bf16 v[96:99], a[24:27], a[136:139], v[96:99]\n\t"
    "s_add_u32 m0, s64, 8192\n\t"
    "v_mfma_f32_16x16x32_bf16 v[96:99], a[28:31], a[140:143], v[96:99]\n\t"
    "buffer_load_dwordx4 v229, s[40:43], 0 offen lds\n\t"
    "v_mfma_f32_16x16x32_bf16 v[100:103], a[32:35], a[136:139], v[100:103]\n\t"
    "ds_read_b64_tr_b16 a[74:75], v226 offset:34048\n\t"
    "v_mfma_f32_16x16x32_bf16 v[100:103], a[36:39], a[140:143], v[100:103]\n\t"
    "v_add_u32 v237, s21, v245\n\t"
    "v_mfma_f32_16x16x32_bf16 v[104:107], a[40:43], a[136:139], v[104:107]\n\t"
    "ds_read_b64_tr_b16 a[80:81], v226 offset:40960\n\t"
    "v_mfma_f32_16x16x32_bf16 v[104:107], a[44:47], a[140:143], v[104:107]\n\t"
    "s_add_u32 m0, s64, 122880\n\t"
    "v_mfma_f32_16x16x32_bf16 v[108:111], a[48:51], a[136:139], v[108:111]\n\t"
    "buffer_load_dwordx4 v231, s[44:47], s61 offen lds\n\t"
    "v_mfma_f32_16x16x32_bf16 v[108:111], a[52:55], a[140:143], v[108:111]\n\t"
    "s_waitcnt lgkmcnt(9)\n\t"
    "v_mfma_f32_16x16x32_bf16 v[112:115], a[0:3], a[112:115], v[112:115]\n\t"
    "ds_read_b128 a[128:131], v224 offset:20480\n\t"
    "v_mfma_f32_16x16x32_bf16 v[112:115], a[4:7], a[116:119], v[112:115]\n\t"
    "ds_read_b128 a[132:135], v225 offset:20480\n\t"
    "v_mfma_f32_16x16x32_bf16 v[116:119], a[8:11], a[112:115], v[116:119]\n\t"
    "ds_read_b64_tr_b16 a[82:83], v226 offset:41984\n\t"
    "v_mfma_f32_16x16x32_bf16 v[116:119], a[12:15], a[116:119], v[116:119]\n\t"
    "s_add_u32 m0, s64, 4096\n\t"
    "v_mfma_f32_16x16x32_bf16 v[120:123], a[16:19], a[112:115], v[120:123]\n\t"
    "buffer_load_dwordx4 v229, s[36:39], s63 offen lds\n\t"
    "v_mfma_f32_16x16x32_bf16 v[120:123], a[20:23], a[116:119], v[120:123]\n\t"
    "s_lshl_b32 s21, s19, 18\n\t"
    "s_and_b32 s22, s68, 1\n\t"
    "v_mfma_f32_16x16x32_bf16 v[124:127], a[24:27], a[112:115], v[124:127]\n\t"
    "ds_read_b64_tr_b16 a[88:89], v227 offset:40960\n\t"
    "v_mfma_f32_16x16x32_bf16 v[124:127], a[28:31], a[116:119], v[124:127]\n\t"
    "s_lshl_b32 s22, s22, 16\n\t"
    "s_add_u32 s21, s21, s22\n\t"
    "v_mfma_f32_16x16x32_bf16 v[128:131], a[32:35], a[112:115], v[128:131]\n\t"
    "ds_read_b64_tr_b16 a[90:91], v227 offset:41984\n\t"
    "v_mfma_f32_16x16x32_bf16 v[128:131], a[36:39], a[116:119], v[128:131]\n\t"
    "s_add_u32 m0, s64, 126976\n\t"
    "v_mfma_f32_16x16x32_bf16 v[132:135], a[40:43], a[112:115], v[132:135]\n\t"
    "buffer_load_dwordx4 v231, s[44:47], s62 offen lds\n\t"
    "v_mfma_f32_16x16x32_bf16 v[132:135], a[44:47], a[116:119], v[132:135]\n\t"
    "s_lshl_b32 s22, s20, 8\n\t"
    "s_add_u32 s21, s21, s22\n\t"
    "v_mfma_f32_16x16x32_bf16 v[136:139], a[48:51], a[112:115], v[136:139]\n\t"
    "s_lshl_b32 s22, s18, 7\n\t"
    "s_add_u32 s21, s21, s22\n\t"
    "v_mfma_f32_16x16x32_bf16 v[136:139], a[52:55], a[116:119], v[136:139]\n\t"
    "ds_read_b64_tr_b16 a[96:97], v226 offset:41216\n\t"
    "s_waitcnt lgkmcnt(9)\n\t"
    "v_mfma_f32_16x16x32_bf16 v[140:143], a[0:3], a[120:123], v[140:143]\n\t"
    "ds_read_b128 a[136:139], v224 offset:22528\n\t"
    "v_mfma_f32_16x16x32_bf16 v[140:143], a[4:7], a[124:127], v[140:143]\n\t"
    "ds_read_b128 a[140:143], v225 offset:22528\n\t"
    "v_mfma_f32_16x16x32_bf16 v[144:147], a[8:11], a[120:123], v[144:147]\n\t"
    "s_add_u32 m0, s64, 12288\n\t"
    "v_mfma_f32_16x16x32_bf16 v[144:147], a[12:15], a[124:127], v[144:147]\n\t"
    "buffer_load_dwordx4 v229, s[40:43], s63 offen lds\n\t"
    "v_mfma_f32_16x16x32_bf16 v[148:151], a[16:19], a[120:123], v[148:151]\n\t"
    "s_andn2_b32 s22, 2, s68\n\t"
    "s_lshl_b32 s22, s22, 5\n\t"
    "v_mfma_f32_16x16x32_bf16 v[148:151], a[20:23], a[124:127], v[148:151]\n\t"
    "ds_read_b64_tr_b16 a[98:99], v226 offset:42240\n\t"
    "v_add_u32 v231, 131072, v230\n\t"
    "v_mfma_f32_16x16x32_bf16 v[152:155], a[24:27], a[120:123], v[152:155]\n\t"
    "s_add_u32 s21, s21, s22\n\t"
    "v_mfma_f32_16x16x32_bf16 v[152:155], a[28:31], a[124:127], v[152:155]\n\t"
    "ds_read_b64_tr_b16 a[104:105], v228 offset:32768\n\t"
    "v_mfma_f32_16x16x32_bf16 v[156:159], a[32:35], a[120:123], v[156:159]\n\t"
    "v_lshrrev_b32 v245, 2, v238\n\t"
    "v_mfma_f32_16x16x32_bf16 v[156:159], a[36:39], a[124:127], v[156:159]\n\t"
    "ds_read_b64_tr_b16 a[106:107], v228 offset:33792\n\t"
    "v_add_u32 v229, 229376, v229\n\t"
    "v_mfma_f32_16x16x32_bf16 v[160:163], a[40:43], a[120:123], v[160:163]\n\t"
    "v_lshlrev_b32 v245, 11, v245\n\t"
    "v_mfma_f32_16x16x32_bf16 v[160:163], a[44:47], a[124:127], v[160:163]\n\t"
    "v_lshl_add_u32 v245, v242, 4, v245\n\t"
    "v_mfma_f32_16x16x32_bf16 v[164:167], a[48:51], a[120:123], v[164:167]\n\t"
    "v_add_u32 v254, s21, v245\n\t"
    "v_mfma_f32_16x16x32_bf16 v[164:167], a[52:55], a[124:127], v[164:167]\n\t"
    "s_waitcnt vmcnt(8) lgkmcnt(9)\n\t"
    "s_barrier\n\t"
    "v_mfma_f32_16x16x32_bf16 v[168:171], a[0:3], a[128:131], v[168:171]\n\t"
    "ds_read_b128 a[112:115], v224 offset:32768\n\t"
    "v_mfma_f32_16x16x32_bf16 v[168:171], a[4:7], a[132:135], v[168:171]\n\t"
    "ds_read_b128 a[116:119], v225 offset:32768\n\t"
    "v_mfma_f32_16x16x32_bf16 v[172:175], a[8:11], a[128:131], v[172:175]\n\t"
    "ds_read_b64_tr_b16 a[60:61], v226 offset:49152\n\t"
    "s_add_u32 m0, s64, 65536\n\t"
    "v_mfma_f32_16x16x32_bf16 v[172:175], a[12:15], a[132:135], v[172:175]\n\t"
    "buffer_load_dwordx4 v230, s[44:47], 0 offen lds\n\t"
    "v_mfma_f32_16x16x32_bf16 v[176:179], a[16:19], a[128:131], v[176:179]\n\t"
    "ds_read_b64_tr_b16 a[62:63], v226 offset:50176\n\t"
    "v_mfma_f32_16x16x32_bf16 v[176:179], a[20:23], a[132:135], v[176:179]\n\t"
    "ds_read_b64_tr_b16 a[68:69], v227 offset:49152\n\t"
    "v_mfma_f32_16x16x32_bf16 v[180:183], a[24:27], a[128:131], v[180:183]\n\t"
    "ds_read_b64_tr_b16 a[70:71], v227 offset:50176\n\t"
    "v_mfma_f32_16x16x32_bf16 v[180:183], a[28:31], a[132:135], v[180:183]\n\t"
    "ds_read_b64_tr_b16 a[76:77], v226 offset:49408\n\t"
    "v_mfma_f32_16x16x32_bf16 v[184:187], a[32:35], a[128:131], v[184:187]\n\t"
    "ST1_%=:\n\t"
    "s_cbranch_execz ST5_%=\n\t"
    "ds_read_b64_tr_b16 a[78:79], v226 offset:50432\n\t"
    "s_add_u32 m0, s64, 16384\n\t"
    "v_mfma_f32_16x16x32_bf16 v[184:187], a[36:39], a[132:135], v[184:187]\n\t"
    "buffer_load_dwordx4 v229, s[36:39], 0 offen lds\n\t"
    "v_mfma_f32_16x16x32_bf16 v[188:191], a[40:43], a[128:131], v[188:191]\n\t"
    "ds_read_b64_tr_b16 a[84:85], v226 offset:57344\n\t"
    "v_mfma_f32_16x16x32_bf16 v[188:191], a[44:47], a[132:135], v[188:191]\n\t"
    "ds_read_b64_tr_b16 a[86:87], v226 offset:58368\n\t"
    "v_mfma_f32_16x16x32_bf16 v[192:195], a[48:51], a[128:131], v[192:195]\n\t"
    "ds_read_b64_tr_b16 a[92:93], v227 offset:57344\n\t"
    "v_mfma_f32_16x16x32_bf16 v[192:195], a[52:55], a[132:135], v[192:195]\n\t"
    "ds_read_b64_tr_b16 a[94:95], v227 offset:58368\n\t"
    "s_waitcnt lgkmcnt(15)\n\t"
    "v_mfma_f32_16x16x32_bf16 v[196:199], a[0:3], a[136:139], v[196:199]\n\t"
    "ds_read_b128 a[120:123], v224 offset:34816\n\t"
    "v_mfma_f32_16x16x32_bf16 v[196:199], a[4:7], a[140:143], v[196:199]\n\t"
    "ds_read_b128 a[124:127], v225 offset:34816\n\t"
    "s_add_u32 m0, s64, 69632\n\t"
    "v_mfma_f32_16x16x32_bf16 v[200:203], a[8:11], a[136:139], v[200:203]\n\t"
    "buffer_load_dwordx4 v230, s[44:47], s60 offen lds\n\t"
    "v_mfma_f32_16x16x32_bf16 v[200:203], a[12:15], a[140:143], v[200:203]\n\t"
    "ds_read_b64_tr_b16 a[100:101], v226 offset:57600\n\t"
    "v_mfma_f32_16x16x32_bf16 v[204:207], a[16:19], a[136:139], v[204:207]\n\t"
    "ds_read_b64_tr_b16 a[102:103], v226 offset:58624\n\t"
    "v_mfma_f32_16x16x32_bf16 v[204:207], a[20:23], a[140:143], v[204:207]\n\t"
    "ds_read_b64_tr_b16 a[108:109], v228 offset:49152\n\t"
    "v_mfma_f32_16x16x32_bf16 v[208:211], a[24:27], a[136:139], v[208:211]\n\t"
    "ds_read_b64_tr_b16 a[110:111], v228 offset:50176\n\t"
    "s_add_u32 m0, s64, 24576\n\t"
    "v_mfma_f32_16x16x32_bf16 v[208:211], a[28:31], a[140:143], v[208:211]\n\t"
    "buffer_load_dwordx4 v229, s[40:43], 0 offen lds\n\t"
    "v_mfma_f32_16x16x32_bf16 v[212:215], a[32:35], a[136:139], v[212:215]\n\t"
    "s_mul_i32 s21, s18, 3670144\n\t"
    "s_lshl_b32 s22, s68, 14\n\t"
    "v_mfma_f32_16x16x32_bf16 v[212:215], a[36:39], a[140:143], v[212:215]\n\t"
    "s_add_u32 s21, s21, s22\n\t"
    "s_lshl_b32 s22, s20, 8\n\t"
    "v_mfma_f32_16x16x32_bf16 v[216:219], a[40:43], a[136:139], v[216:219]\n\t"
    "s_add_u32 s21, s21, s22\n\t"
    "s_and_b32 s22, s68, 1\n\t"
    "v_mfma_f32_16x16x32_bf16 v[216:219], a[44:47], a[140:143], v[216:219]\n\t"
    "s_add_u32 m0, s64, 73728\n\t"
    "v_mfma_f32_16x16x32_bf16 v[220:223], a[48:51], a[136:139], v[220:223]\n\t"
    "buffer_load_dwordx4 v230, s[44:47], s61 offen lds\n\t"
    "v_mfma_f32_16x16x32_bf16 v[220:223], a[52:55], a[140:143], v[220:223]\n\t"
    "s_waitcnt lgkmcnt(0)\n\t"
    "v_mfma_f32_16x16x32_bf16 v[0:3], a[56:59], a[112:115], v[0:3]\n\t"
    "ds_read_b128 a[128:131], v224 offset:36864\n\t"
    "v_mfma_f32_16x16x32_bf16 v[0:3], a[60:63], a[116:119], v[0:3]\n\t"
    "ds_read_b128 a[132:135], v225 offset:36864\n\t"
    "v_mfma_f32_16x16x32_bf16 v[4:7], a[64:67], a[112:115], v[4:7]\n\t"
    "s_lshl_b32 s22, s22, 1\n\t"
    "v_mfma_f32_16x16x32_bf16 v[4:7], a[68:71], a[116:119], v[4:7]\n\t"
    "s_add_u32 m0, s64, 20480\n\t"
    "v_mfma_f32_16x16x32_bf16 v[8:11], a[72:75], a[112:115], v[8:11]\n\t"
    "buffer_load_dwordx4 v229, s[36:39], s63 offen lds\n\t"
    "v_mfma_f32_16x16x32_bf16 v[8:11], a[76:79], a[116:119], v[8:11]\n\t"
    "v_lshl_add_u32 v245, v244, 2, v241\n\t"
    "v_mfma_f32_16x16x32_bf16 v[12:15], a[80:83], a[112:115], v[12:15]\n\t"
    "v_lshlrev_b32 v245, 11, v245\n\t"
    "v_mfma_f32_16x16x32_bf16 v[12:15], a[84:87], a[116:119], v[12:15]\n\t"
    "v_lshl_add_u32 v245, v243, 6, v245\n\t"
    "v_mfma_f32_16x16x32_bf16 v[16:19], a[88:91], a[112:115], v[16:19]\n\t"
    "v_xor_b32 v243, s22, v242\n\t"
    "v_mfma_f32_16x16x32_bf16 v[16:19], a[92:95], a[116:119], v[16:19]\n\t"
    "s_add_u32 m0, s64, 77824\n\t"
    "v_mfma_f32_16x16x32_bf16 v[20:23], a[96:99], a[112:115], v[20:23]\n\t"
    "buffer_load_dwordx4 v230, s[44:47], s62 offen lds\n\t"
    "v_mfma_f32_16x16x32_bf16 v[20:23], a[100:103], a[116:119], v[20:23]\n\t"
    "v_lshl_add_u32 v245, v243, 4, v245\n\t"
    "v_mfma_f32_16x16x32_bf16 v[24:27], a[104:107], a[112:115], v[24:27]\n\t"
    "v_add_u32 v247, s21, v245\n\t"
    "v_mfma_f32_16x16x32_bf16 v[24:27], a[108:111], a[116:119], v[24:27]\n\t"
    "v_mfma_f32_16x16x32_bf16 v[28:31], a[56:59], a[120:123], v[28:31]\n\t"
    "ds_read_b128 a[136:139], v224 offset:38912\n\t"
    "v_mfma_f32_16x16x32_bf16 v[28:31], a[60:63], a[124:127], v[28:31]\n\t"
    "ds_read_b128 a[140:143], v225 offset:38912\n\t"
    "v_mfma_f32_16x16x32_bf16 v[32:35], a[64:67], a[120:123], v[32:35]\n\t"
    "s_add_u32 m0, s64, 28672\n\t"
    "v_mfma_f32_16x16x32_bf16 v[32:35], a[68:71], a[124:127], v[32:35]\n\t"
    "buffer_load_dwordx4 v229, s[40:43], s63 offen lds\n\t"
    "v_mfma_f32_16x16x32_bf16 v[36:39], a[72:75], a[120:123], v[36:39]\n\t"
    "s_cmp_eq_u32 s65, 0\n\t"
    "s_cbranch_scc1 SMO%=\n\t"
    "SMS%=:\n\t"
    "v_mfma_f32_16x16x32_bf16 v[36:39], a[76:79], a[124:127], v[36:39]\n\t"
    "s_lshr_b64 s[72:73], s[72:73], 4\n\t"
    "s_and_b32 s66, s72, 15\n\t"
    "v_mfma_f32_16x16x32_bf16 v[40:43], a[80:83], a[120:123], v[40:43]\n\t"
    "v_xor_b32 v246, 128, v247\n\t"
    "v_mfma_f32_16x16x32_bf16 v[40:43], a[84:87], a[124:127], v[40:43]\n\t"
    "v_lshl_add_u32 v230, s66, 7, v233\n\t"
    "v_mfma_f32_16x16x32_bf16 v[44:47], a[88:91], a[120:123], v[44:47]\n\t"
    "s_lshl_b32 s21, s19, 2\n\t"
    "s_and_b32 s22, s68, 1\n\t"
    "v_mfma_f32_16x16x32_bf16 v[44:47], a[92:95], a[124:127], v[44:47]\n\t"
    "s_lshr_b64 s[74:75], s[74:75], 4\n\t"
    "s_and_b32 s71, s74, 15\n\t"
    "v_mfma_f32_16x16x32_bf16 v[48:51], a[96:99], a[120:123], v[48:51]\n\t"
    "s_add_u32 s21, s21, s22\n\t"
    "s_mul_i32 s21, s21, 229376\n\t"
    "v_mfma_f32_16x16x32_bf16 v[48:51], a[100:103], a[124:127], v[48:51]\n\t"
    "s_mul_i32 s71, s71, 458752\n\t"
    "v_mfma_f32_16x16x32_bf16 v[52:55], a[104:107], a[120:123], v[52:55]\n\t"
    "s_mul_i32 s22, s18, 57344\n\t"
    "s_add_u32 s21, s21, s22\n\t"
    "v_mfma_f32_16x16x32_bf16 v[52:55], a[108:111], a[124:127], v[52:55]\n\t"
    "v_add_u32 v229, s71, v232\n\t"
    "s_waitcnt vmcnt(8) lgkmcnt(2)\n\t"
    "s_barrier\n\t"
    "v_mfma_f32_16x16x32_bf16 v[56:59], a[56:59], a[128:131], v[56:59]\n\t"
    "ds_read_b128 a[112:115], v224 offset:49152\n\t"
    "v_mfma_f32_16x16x32_bf16 v[56:59], a[60:63], a[132:135], v[56:59]\n\t"
    "ds_read_b128 a[116:119], v225 offset:49152\n\t"
    "v_mfma_f32_16x16x32_bf16 v[60:63], a[64:67], a[128:131], v[60:63]\n\t"
    "s_add_u32 m0, s64, 81920\n\t"
    "v_mfma_f32_16x16x32_bf16 v[60:63], a[68:71], a[132:135], v[60:63]\n\t"
    "buffer_load_dwordx4 v231, s[44:47], 0 offen lds\n\t"
    "v_mfma_f32_16x16x32_bf16 v[64:67], a[72:75], a[128:131], v[64:67]\n\t"
    "ds_read_b64_tr_b16 a[0:1], v226 offset:0\n\t"
    "v_mfma_f32_16x16x32_bf16 v[64:67], a[76:79], a[132:135], v[64:67]\n\t"
    "s_and_b32 s22, s68, 2\n\t"
    "s_lshl_b32 s22, s22, 10\n\t"
    "v_mfma_f32_16x16x32_bf16 v[68:71], a[80:83], a[128:131], v[68:71]\n\t"
    "ds_read_b64_tr_b16 a[2:3], v226 offset:1024\n\t"
    "v_mfma_f32_16x16x32_bf16 v[68:71], a[84:87], a[132:135], v[68:71]\n\t"
    "s_add_u32 s21, s21, s22\n\t"
    "v_mfma_f32_16x16x32_bf16 v[72:75], a[88:91], a[128:131], v[72:75]\n\t"
    "s_add_u32 m0, s64, 32768\n\t"
    "v_mfma_f32_16x16x32_bf16 v[72:75], a[92:95], a[132:135], v[72:75]\n\t"
    "buffer_load_dwordx4 v229, s[36:39], 0 offen lds\n\t"
    "v_mfma_f32_16x16x32_bf16 v[76:79], a[96:99], a[128:131], v[76:79]\n\t"
    "ds_read_b64_tr_b16 a[8:9], v227 offset:0\n\t"
    "v_mfma_f32_16x16x32_bf16 v[76:79], a[100:103], a[132:135], v[76:79]\n\t"
    "v_lshl_add_u32 v252, v238, 4, s21\n\t"
    "v_mfma_f32_16x16x32_bf16 v[80:83], a[104:107], a[128:131], v[80:83]\n\t"
    "ds_read_b64_tr_b16 a[10:11], v227 offset:1024\n\t"
    "v_mfma_f32_16x16x32_bf16 v[80:83], a[108:111], a[132:135], v[80:83]\n\t"
    "s_waitcnt lgkmcnt(6)\n\t"
    "v_mfma_f32_16x16x32_bf16 v[84:87], a[56:59], a[136:139], v[84:87]\n\t"
    "ds_read_b128 a[120:123], v224 offset:51200\n\t"
    "v_mfma_f32_16x16x32_bf16 v[84:87], a[60:63], a[140:143], v[84:87]\n\t"
    "ds_read_b128 a[124:127], v225 offset:51200\n\t"
    "s_add_u32 m0, s64, 86016\n\t"
    "v_mfma_f32_16x16x32_bf16 v[88:91], a[64:67], a[136:139], v[88:91]\n\t"
    "buffer_load_dwordx4 v231, s[44:47], s60 offen lds\n\t"
    "v_mfma_f32_16x16x32_bf16 v[88:91], a[68:71], a[140:143], v[88:91]\n\t"
    "ds_read_b64_tr_b16 a[16:17], v226 offset:256\n\t"
    "v_mfma_f32_16x16x32_bf16 v[92:95], a[72:75], a[136:139], v[92:95]\n\t"
    "s_mov_b32 s48, s10\n\t"
    "s_and_b32 s49, s11, 0xffff\n\t"
    "v_mfma_f32_16x16x32_bf16 v[92:95], a[76:79], a[140:143], v[92:95]\n\t"
    "s_mov_b32 s50, 7340032\n\t"
    "s_mov_b32 s51, 0x20000\n\t"
    "v_mfma_f32_16x16x32_bf16 v[96:99], a[80:83], a[136:139], v[96:99]\n\t"
    "s_add_u32 m0, s64, 40960\n\t"
    "v_mfma_f32_16x16x32_bf16 v[96:99], a[84:87], a[140:143], v[96:99]\n\t"
    "buffer_load_dwordx4 v229, s[40:43], 0 offen lds\n\t"
    "v_mfma_f32_16x16x32_bf16 v[100:103], a[88:91], a[136:139], v[100:103]\n\t"
    "ds_read_b64_tr_b16 a[18:19], v226 offset:1280\n\t"
    "v_mfma_f32_16x16x32_bf16 v[100:103], a[92:95], a[140:143], v[100:103]\n\t"
    "s_mov_b32 s56, s14\n\t"
    "s_and_b32 s57, s15, 0xffff\n\t"
    "v_mfma_f32_16x16x32_bf16 v[104:107], a[96:99], a[136:139], v[104:107]\n\t"
    "ds_read_b64_tr_b16 a[24:25], v226 offset:8192\n\t"
    "v_mfma_f32_16x16x32_bf16 v[104:107], a[100:103], a[140:143], v[104:107]\n\t"
    "s_add_u32 m0, s64, 90112\n\t"
    "v_mfma_f32_16x16x32_bf16 v[108:111], a[104:107], a[136:139], v[108:111]\n\t"
    "buffer_load_dwordx4 v231, s[44:47], s61 offen lds\n\t"
    "v_mfma_f32_16x16x32_bf16 v[108:111], a[108:111], a[140:143], v[108:111]\n\t"
    "s_waitcnt lgkmcnt(9)\n\t"
    "v_mfma_f32_16x16x32_bf16 v[112:115], a[56:59], a[112:115], v[112:115]\n\t"
    "ds_read_b128 a[128:131], v224 offset:53248\n\t"
    "v_mfma_f32_16x16x32_bf16 v[112:115], a[60:63], a[116:119], v[112:115]\n\t"
    "ds_read_b128 a[132:135], v225 offset:53248\n\t"
    "v_mfma_f32_16x16x32_bf16 v[116:119], a[64:67], a[112:115], v[116:119]\n\t"
    "ds_read_b64_tr_b16 a[26:27], v226 offset:9216\n\t"
    "v_mfma_f32_16x16x32_bf16 v[116:119], a[68:71], a[116:119], v[116:119]\n\t"
    "s_add_u32 m0, s64, 36864\n\t"
    "v_mfma_f32_16x16x32_bf16 v[120:123], a[72:75], a[112:115], v[120:123]\n\t"
    "buffer_load_dwordx4 v229, s[36:39], s63 offen lds\n\t"
    "v_mfma_f32_16x16x32_bf16 v[120:123], a[76:79], a[116:119], v[120:123]\n\t"
    "s_mov_b32 s58, 4194304\n\t"
    "s_mov_b32 s59, 0x20000\n\t"
    "v_mfma_f32_16x16x32_bf16 v[124:127], a[80:83], a[112:115], v[124:127]\n\t"
    "ds_read_b64_tr_b16 a[32:33], v227 offset:8192\n\t"
    "v_mfma_f32_16x16x32_bf16 v[124:127], a[84:87], a[116:119], v[124:127]\n\t"
    "s_mov_b32 s69, 1048576\n\t"
    "s_mov_b32 s78, 0xbfb8aa3b\n\t"
    "v_mfma_f32_16x16x32_bf16 v[128:131], a[88:91], a[112:115], v[128:131]\n\t"
    "ds_read_b64_tr_b16 a[34:35], v227 offset:9216\n\t"
    "v_mfma_f32_16x16x32_bf16 v[128:131], a[92:95], a[116:119], v[128:131]\n\t"
    "s_add_u32 m0, s64, 94208\n\t"
    "v_mfma_f32_16x16x32_bf16 v[132:135], a[96:99], a[112:115], v[132:135]\n\t"
    "buffer_load_dwordx4 v231, s[44:47], s62 offen lds\n\t"
    "v_mfma_f32_16x16x32_bf16 v[132:135], a[100:103], a[116:119], v[132:135]\n\t"
    "s_mov_b32 s79, 0xbfb8aa3b\n\t"
    "v_mfma_f32_16x16x32_bf16 v[136:139], a[104:107], a[112:115], v[136:139]\n\t"
    "v_mov_b32 v255, 1\n\t"
    "v_mfma_f32_16x16x32_bf16 v[136:139], a[108:111], a[116:119], v[136:139]\n\t"
    "ds_read_b64_tr_b16 a[40:41], v226 offset:8448\n\t"
    "s_waitcnt lgkmcnt(9)\n\t"
    "v_mfma_f32_16x16x32_bf16 v[140:143], a[56:59], a[120:123], v[140:143]\n\t"
    "ds_read_b128 a[136:139], v224 offset:55296\n\t"
    "v_mfma_f32_16x16x32_bf16 v[140:143], a[60:63], a[124:127], v[140:143]\n\t"
    "ds_read_b128 a[140:143], v225 offset:55296\n\t"
    "v_mfma_f32_16x16x32_bf16 v[144:147], a[64:67], a[120:123], v[144:147]\n\t"
    "s_add_u32 m0, s64, 45056\n\t"
    "v_mfma_f32_16x16x32_bf16 v[144:147], a[68:71], a[124:127], v[144:147]\n\t"
    "buffer_load_dwordx4 v229, s[40:43], s63 offen lds\n\t"
    "v_mfma_f32_16x16x32_bf16 v[148:151], a[72:75], a[120:123], v[148:151]\n\t"
    "v_mfma_f32_16x16x32_bf16 v[148:151], a[76:79], a[124:127], v[148:151]\n\t"
    "ds_read_b64_tr_b16 a[42:43], v226 offset:9472\n\t"
    "v_add_u32 v231, 131072, v230\n\t"
    "v_mfma_f32_16x16x32_bf16 v[152:155], a[80:83], a[120:123], v[152:155]\n\t"
    "s_lshl_b32 s67, s65, 10\n\t"
    "v_mfma_f32_16x16x32_bf16 v[152:155], a[84:87], a[124:127], v[152:155]\n\t"
    "ds_read_b64_tr_b16 a[48:49], v228 offset:0\n\t"
    "v_mfma_f32_16x16x32_bf16 v[156:159], a[88:91], a[120:123], v[156:159]\n\t"
    "buffer_store_dwordx4 a[148:151], v253, s[52:55], s67 offen sc1\n\t"
    "v_mfma_f32_16x16x32_bf16 v[156:159], a[92:95], a[124:127], v[156:159]\n\t"
    "ds_read_b64_tr_b16 a[50:51], v228 offset:1024\n\t"
    "v_add_u32 v229, 229376, v229\n\t"
    "v_mfma_f32_16x16x32_bf16 v[160:163], a[96:99], a[120:123], v[160:163]\n\t"
    "v_mfma_f32_16x16x32_bf16 v[160:163], a[100:103], a[124:127], v[160:163]\n\t"
    "v_mfma_f32_16x16x32_bf16 v[164:167], a[104:107], a[120:123], v[164:167]\n\t"
    "v_mfma_f32_16x16x32_bf16 v[164:167], a[108:111], a[124:127], v[164:167]\n\t"
    "s_waitcnt vmcnt(9) lgkmcnt(9)\n\t"
    "s_barrier\n\t"
    "v_mfma_f32_16x16x32_bf16 v[168:171], a[56:59], a[128:131], v[168:171]\n\t"
    "ds_read_b128 a[112:115], v224 offset:0\n\t"
    "v_mfma_f32_16x16x32_bf16 v[168:171], a[60:63], a[132:135], v[168:171]\n\t"
    "ds_read_b128 a[116:119], v225 offset:0\n\t"
    "v_mfma_f32_16x16x32_bf16 v[172:175], a[64:67], a[128:131], v[172:175]\n\t"
    "ds_read_b64_tr_b16 a[4:5], v226 offset:16384\n\t"
    "s_add_u32 m0, s64, 98304\n\t"
    "v_mfma_f32_16x16x32_bf16 v[172:175], a[68:71], a[132:135], v[172:175]\n\t"
    "buffer_load_dwordx4 v230, s[44:47], 0 offen lds\n\t"
    "v_mfma_f32_16x16x32_bf16 v[176:179], a[72:75], a[128:131], v[176:179]\n\t"
    "ds_read_b64_tr_b16 a[6:7], v226 offset:17408\n\t"
    "v_mfma_f32_16x16x32_bf16 v[176:179], a[76:79], a[132:135], v[176:179]\n\t"
    "ds_read_b64_tr_b16 a[12:13], v227 offset:16384\n\t"
    "v_mfma_f32_16x16x32_bf16 v[180:183], a[80:83], a[128:131], v[180:183]\n\t"
    "ds_read_b64_tr_b16 a[14:15], v227 offset:17408\n\t"
    "v_mfma_f32_16x16x32_bf16 v[180:183], a[84:87], a[132:135], v[180:183]\n\t"
    "ds_read_b64_tr_b16 a[20:21], v226 offset:16640\n\t"
    "v_mfma_f32_16x16x32_bf16 v[184:187], a[88:91], a[128:131], v[184:187]\n\t"
    "ds_read_b64_tr_b16 a[22:23], v226 offset:17664\n\t"
    "s_add_u32 m0, s64, 49152\n\t"
    "v_mfma_f32_16x16x32_bf16 v[184:187], a[92:95], a[132:135], v[184:187]\n\t"
    "buffer_load_dwordx4 v229, s[36:39], 0 offen lds\n\t"
    "v_mfma_f32_16x16x32_bf16 v[188:191], a[96:99], a[128:131], v[188:191]\n\t"
    "ds_read_b64_tr_b16 a[28:29], v226 offset:24576\n\t"
    "v_mfma_f32_16x16x32_bf16 v[188:191], a[100:103], a[132:135], v[188:191]\n\t"
    "ds_read_b64_tr_b16 a[30:31], v226 offset:25600\n\t"
    "v_mfma_f32_16x16x32_bf16 v[192:195], a[104:107], a[128:131], v[192:195]\n\t"
    "ds_read_b64_tr_b16 a[36:37], v227 offset:24576\n\t"
    "v_mfma_f32_16x16x32_bf16 v[192:195], a[108:111], a[132:135], v[192:195]\n\t"
    "ds_read_b64_tr_b16 a[38:39], v227 offset:25600\n\t"
    "s_waitcnt lgkmcnt(15)\n\t"
    "v_mfma_f32_16x16x32_bf16 v[196:199], a[56:59], a[136:139], v[196:199]\n\t"
    "ds_read_b128 a[120:123], v224 offset:2048\n\t"
    "v_mfma_f32_16x16x32_bf16 v[196:199], a[60:63], a[140:143], v[196:199]\n\t"
    "ds_read_b128 a[124:127], v225 offset:2048\n\t"
    "s_add_u32 m0, s64, 102400\n\t"
    "v_mfma_f32_16x16x32_bf16 v[200:203], a[64:67], a[136:139], v[200:203]\n\t"
    "buffer_load_dwordx4 v230, s[44:47], s60 offen lds\n\t"
    "v_mfma_f32_16x16x32_bf16 v[200:203], a[68:71], a[140:143], v[200:203]\n\t"
    "ds_read_b64_tr_b16 a[44:45], v226 offset:24832\n\t"
    "v_mfma_f32_16x16x32_bf16 v[204:207], a[72:75], a[136:139], v[204:207]\n\t"
    "ds_read_b64_tr_b16 a[46:47], v226 offset:25856\n\t"
    "v_mfma_f32_16x16x32_bf16 v[204:207], a[76:79], a[140:143], v[204:207]\n\t"
    "ds_read_b64_tr_b16 a[52:53], v228 offset:16384\n\t"
    "v_mfma_f32_16x16x32_bf16 v[208:211], a[80:83], a[136:139], v[208:211]\n\t"
    "ds_read_b64_tr_b16 a[54:55], v228 offset:17408\n\t"
    "s_add_u32 m0, s64, 57344\n\t"
    "v_mfma_f32_16x16x32_bf16 v[208:211], a[84:87], a[140:143], v[208:211]\n\t"
    "buffer_load_dwordx4 v229, s[40:43], 0 offen lds\n\t"
    "v_mfma_f32_16x16x32_bf16 v[212:215], a[88:91], a[136:139], v[212:215]\n\t"
    "s_add_u32 s65, s65, 1\n\t"
    "v_mfma_f32_16x16x32_bf16 v[212:215], a[92:95], a[140:143], v[212:215]\n\t"
    "v_mfma_f32_16x16x32_bf16 v[216:219], a[96:99], a[136:139], v[216:219]\n\t"
    "v_mfma_f32_16x16x32_bf16 v[216:219], a[100:103], a[140:143], v[216:219]\n\t"
    "s_add_u32 m0, s64, 106496\n\t"
    "v_mfma_f32_16x16x32_bf16 v[220:223], a[104:107], a[136:139], v[220:223]\n\t"
    "buffer_load_dwordx4 v230, s[44:47], s61 offen lds\n\t"
    "s_cmp_lt_u32 s65, 7\n\t"
    "v_mfma_f32_16x16x32_bf16 v[220:223], a[108:111], a[140:143], v[220:223]\n\t"
    "s_cbranch_scc1 LA%=\n\t"
    "s_waitcnt vmcnt(6) lgkmcnt(0)\n\t"
    "s_barrier\n\t"
    "v_mfma_f32_16x16x32_bf16 v[0:3], a[0:3], a[112:115], v[0:3]\n\t"
    "ds_read_b128 a[128:131], v224 offset:4096\n\t"
    "v_mfma_f32_16x16x32_bf16 v[0:3], a[4:7], a[116:119], v[0:3]\n\t"
    "ds_read_b128 a[132:135], v225 offset:4096\n\t"
    "v_mfma_f32_16x16x32_bf16 v[4:7], a[8:11], a[112:115], v[4:7]\n\t"
    "s_add_u32 m0, s64, 53248\n\t"
    "ds_read_b64_tr_b16 a[56:57], v226 offset:32768\n\t"
    "v_mfma_f32_16x16x32_bf16 v[4:7], a[12:15], a[116:119], v[4:7]\n\t"
    "buffer_load_dwordx4 v229, s[36:39], s63 offen lds\n\t"
    "v_mfma_f32_16x16x32_bf16 v[8:11], a[16:19], a[112:115], v[8:11]\n\t"
    "ds_read_b64_tr_b16 a[58:59], v226 offset:33792\n\t"
    "v_mfma_f32_16x16x32_bf16 v[8:11], a[20:23], a[116:119], v[8:11]\n\t"
    "s_add_u32 m0, s64, 110592\n\t"
    "v_mfma_f32_16x16x32_bf16 v[12:15], a[24:27], a[112:115], v[12:15]\n\t"
    "buffer_load_dwordx4 v230, s[44:47], s62 offen lds\n\t"
    "v_mfma_f32_16x16x32_bf16 v[12:15], a[28:31], a[116:119], v[12:15]\n\t"
    "ds_read_b64_tr_b16 a[64:65], v227 offset:32768\n\t"
    "v_mfma_f32_16x16x32_bf16 v[16:19], a[32:35], a[112:115], v[16:19]\n\t"
    "s_add_u32 m0, s64, 61440\n\t"
    "v_mfma_f32_16x16x32_bf16 v[16:19], a[36:39], a[116:119], v[16:19]\n\t"
    "buffer_load_dwordx4 v229, s[40:43], s63 offen lds\n\t"
    "v_mfma_f32_16x16x32_bf16 v[20:23], a[40:43], a[112:115], v[20:23]\n\t"
    "ds_read_b64_tr_b16 a[66:67], v227 offset:33792\n\t"
    "v_mfma_f32_16x16x32_bf16 v[20:23], a[44:47], a[116:119], v[20:23]\n\t"
    "v_mfma_f32_16x16x32_bf16 v[24:27], a[48:51], a[112:115], v[24:27]\n\t"
    "ds_read_b64_tr_b16 a[72:73], v226 offset:33024\n\t"
    "v_xor_b32 v232, 64, v246\n\t"
    "v_mfma_f32_16x16x32_bf16 v[24:27], a[52:55], a[116:119], v[24:27]\n\t"
    "v_xor_b32 v233, 64, v247\n\t"
    "v_mfma_f32_16x16x32_bf16 v[28:31], a[0:3], a[120:123], v[28:31]\n\t"
    "ds_read_b128 a[136:139], v224 offset:6144\n\t"
    "v_mfma_f32_16x16x32_bf16 v[28:31], a[4:7], a[124:127], v[28:31]\n\t"
    "ds_read_b128 a[140:143], v225 offset:6144\n\t"
    "v_mfma_f32_16x16x32_bf16 v[32:35], a[8:11], a[120:123], v[32:35]\n\t"
    "ds_read_b64_tr_b16 a[74:75], v226 offset:34048\n\t"
    "v_mov_b32 v235, v249\n\t"
    "v_mfma_f32_16x16x32_bf16 v[32:35], a[12:15], a[124:127], v[32:35]\n\t"
    "s_add_u32 m0, s64, 114688\n\t"
    "v_mfma_f32_16x16x32_bf16 v[36:39], a[16:19], a[120:123], v[36:39]\n\t"
    "ds_read_b64_tr_b16 a[80:81], v226 offset:40960\n\t"
    "buffer_load_dwordx4 v231, s[44:47], 0 offen lds\n\t"
    "v_mfma_f32_16x16x32_bf16 v[36:39], a[20:23], a[124:127], v[36:39]\n\t"
    "v_mfma_f32_16x16x32_bf16 v[40:43], a[24:27], a[120:123], v[40:43]\n\t"
    "ds_read_b64_tr_b16 a[82:83], v226 offset:41984\n\t"
    "v_mfma_f32_16x16x32_bf16 v[40:43], a[28:31], a[124:127], v[40:43]\n\t"
    "s_add_u32 m0, s64, 118784\n\t"
    "v_mfma_f32_16x16x32_bf16 v[44:47], a[32:35], a[120:123], v[44:47]\n\t"
    "ds_read_b64_tr_b16 a[88:89], v227 offset:40960\n\t"
    "buffer_load_dwordx4 v231, s[44:47], s60 offen lds\n\t"
    "v_mfma_f32_16x16x32_bf16 v[44:47], a[36:39], a[124:127], v[44:47]\n\t"
    "v_mfma_f32_16x16x32_bf16 v[48:51], a[40:43], a[120:123], v[48:51]\n\t"
    "ds_read_b64_tr_b16 a[90:91], v227 offset:41984\n\t"
    "v_mfma_f32_16x16x32_bf16 v[48:51], a[44:47], a[124:127], v[48:51]\n\t"
    "s_add_u32 m0, s64, 122880\n\t"
    "v_mfma_f32_16x16x32_bf16 v[52:55], a[48:51], a[120:123], v[52:55]\n\t"
    "ds_read_b64_tr_b16 a[96:97], v226 offset:41216\n\t"
    "buffer_load_dwordx4 v231, s[44:47], s61 offen lds\n\t"
    "v_mfma_f32_16x16x32_bf16 v[52:55], a[52:55], a[124:127], v[52:55]\n\t"
    "s_waitcnt lgkmcnt(13)\n\t"
    "v_mfma_f32_16x16x32_bf16 v[56:59], a[0:3], a[128:131], v[56:59]\n\t"
    "ds_read_b128 a[112:115], v224 offset:16384\n\t"
    "v_mfma_f32_16x16x32_bf16 v[56:59], a[4:7], a[132:135], v[56:59]\n\t"
    "ds_read_b128 a[116:119], v225 offset:16384\n\t"
    "v_mfma_f32_16x16x32_bf16 v[60:63], a[8:11], a[128:131], v[60:63]\n\t"
    "ds_read_b64_tr_b16 a[98:99], v226 offset:42240\n\t"
    "v_mfma_f32_16x16x32_bf16 v[60:63], a[12:15], a[132:135], v[60:63]\n\t"
    "s_add_u32 m0, s64, 126976\n\t"
    "v_mfma_f32_16x16x32_bf16 v[64:67], a[16:19], a[128:131], v[64:67]\n\t"
    "ds_read_b64_tr_b16 a[104:105], v228 offset:32768\n\t"
    "buffer_load_dwordx4 v231, s[44:47], s62 offen lds\n\t"
    "v_mfma_f32_16x16x32_bf16 v[64:67], a[20:23], a[132:135], v[64:67]\n\t"
    "v_mfma_f32_16x16x32_bf16 v[68:71], a[24:27], a[128:131], v[68:71]\n\t"
    "ds_read_b64_tr_b16 a[106:107], v228 offset:33792\n\t"
    "s_mov_b32 s67, 7168\n\t"
    "v_mfma_f32_16x16x32_bf16 v[68:71], a[28:31], a[132:135], v[68:71]\n\t"
    "buffer_store_dwordx4 a[148:151], v253, s[52:55], s67 offen sc1\n\t"
    "v_mfma_f32_16x16x32_bf16 v[72:75], a[32:35], a[128:131], v[72:75]\n\t"
    "v_add_u32 v230, 114688, v252\n\t"
    "v_mfma_f32_16x16x32_bf16 v[72:75], a[36:39], a[132:135], v[72:75]\n\t"
    "v_xor_b32 v231, 32768, v253\n\t"
    "s_mov_b32 s66, 0\n\t"
    "v_mfma_f32_16x16x32_bf16 v[76:79], a[40:43], a[128:131], v[76:79]\n\t"
    "s_add_u32 m0, s64, 131072\n\t"
    "v_mfma_f32_16x16x32_bf16 v[76:79], a[44:47], a[132:135], v[76:79]\n\t"
    "buffer_load_dwordx4 v246, s[48:51], s66 offen lds\n\t"
    "v_mfma_f32_16x16x32_bf16 v[80:83], a[48:51], a[128:131], v[80:83]\n\t"
    "v_mfma_f32_16x16x32_bf16 v[80:83], a[52:55], a[132:135], v[80:83]\n\t"
    "s_waitcnt vmcnt(6) lgkmcnt(0)\n\t"
    "s_barrier\n\t"
    "v_mfma_f32_16x16x32_bf16 v[84:87], a[0:3], a[136:139], v[84:87]\n\t"
    "ds_read_b128 a[120:123], v224 offset:32768\n\t"
    "v_mfma_f32_16x16x32_bf16 v[84:87], a[4:7], a[140:143], v[84:87]\n\t"
    "ds_read_b128 a[124:127], v225 offset:32768\n\t"
    "v_mfma_f32_16x16x32_bf16 v[88:91], a[8:11], a[136:139], v[88:91]\n\t"
    "ds_read_b64_tr_b16 a[60:61], v226 offset:49152\n\t"
    "s_mov_b32 s66, 65536\n\t"
    "v_mfma_f32_16x16x32_bf16 v[88:91], a[12:15], a[140:143], v[88:91]\n\t"
    "ds_read_b64_tr_b16 a[62:63], v226 offset:50176\n\t"
    "s_add_u32 m0, s64, 135168\n\t"
    "v_mfma_f32_16x16x32_bf16 v[92:95], a[16:19], a[136:139], v[92:95]\n\t"
    "ds_read_b64_tr_b16 a[68:69], v227 offset:49152\n\t"
    "buffer_load_dwordx4 v246, s[48:51], s66 offen lds\n\t"
    "v_mfma_f32_16x16x32_bf16 v[92:95], a[20:23], a[140:143], v[92:95]\n\t"
    "ds_read_b64_tr_b16 a[70:71], v227 offset:50176\n\t"
    "s_mov_b32 s66, 131072\n\t"
    "v_mfma_f32_16x16x32_bf16 v[96:99], a[24:27], a[136:139], v[96:99]\n\t"
    "ds_read_b64_tr_b16 a[76:77], v226 offset:49408\n\t"
    "s_add_u32 m0, s64, 139264\n\t"
    "v_mfma_f32_16x16x32_bf16 v[96:99], a[28:31], a[140:143], v[96:99]\n\t"
    "ds_read_b64_tr_b16 a[78:79], v226 offset:50432\n\t"
    "buffer_load_dwordx4 v232, s[48:51], s66 offen lds\n\t"
    "v_mfma_f32_16x16x32_bf16 v[100:103], a[32:35], a[136:139], v[100:103]\n\t"
    "ds_read_b64_tr_b16 a[84:85], v226 offset:57344\n\t"
    "s_mov_b32 s66, 196608\n\t"
    "v_mfma_f32_16x16x32_bf16 v[100:103], a[36:39], a[140:143], v[100:103]\n\t"
    "ds_read_b64_tr_b16 a[86:87], v226 offset:58368\n\t"
    "s_add_u32 m0, s64, 143360\n\t"
    "v_mfma_f32_16x16x32_bf16 v[104:107], a[40:43], a[136:139], v[104:107]\n\t"
    "ds_read_b64_tr_b16 a[92:93], v227 offset:57344\n\t"
    "buffer_load_dwordx4 v232, s[48:51], s66 offen lds\n\t"
    "v_mfma_f32_16x16x32_bf16 v[104:107], a[44:47], a[140:143], v[104:107]\n\t"
    "ds_read_b64_tr_b16 a[94:95], v227 offset:58368\n\t"
    "s_mov_b32 s66, 0\n\t"
    "v_mfma_f32_16x16x32_bf16 v[108:111], a[48:51], a[136:139], v[108:111]\n\t"
    "ds_read_b64_tr_b16 a[100:101], v226 offset:57600\n\t"
    "s_add_u32 m0, s64, 147456\n\t"
    "v_mfma_f32_16x16x32_bf16 v[108:111], a[52:55], a[140:143], v[108:111]\n\t"
    "ds_read_b64_tr_b16 a[102:103], v226 offset:58624\n\t"
    "buffer_load_dwordx4 v247, s[48:51], s66 offen lds\n\t"
    "v_mfma_f32_16x16x32_bf16 v[112:115], a[0:3], a[112:115], v[112:115]\n\t"
    "ds_read_b128 a[128:131], v224 offset:34816\n\t"
    "v_mfma_f32_16x16x32_bf16 v[112:115], a[4:7], a[116:119], v[112:115]\n\t"
    "ds_read_b128 a[132:135], v225 offset:34816\n\t"
    "v_mfma_f32_16x16x32_bf16 v[116:119], a[8:11], a[112:115], v[116:119]\n\t"
    "ds_read_b64_tr_b16 a[108:109], v228 offset:49152\n\t"
    "s_mov_b32 s66, 65536\n\t"
    "v_mfma_f32_16x16x32_bf16 v[116:119], a[12:15], a[116:119], v[116:119]\n\t"
    "ds_read_b64_tr_b16 a[110:111], v228 offset:50176\n\t"
    "s_add_u32 m0, s64, 151552\n\t"
    "v_mfma_f32_16x16x32_bf16 v[120:123], a[16:19], a[112:115], v[120:123]\n\t"
    "buffer_load_dwordx4 v247, s[48:51], s66 offen lds\n\t"
    "v_mfma_f32_16x16x32_bf16 v[120:123], a[20:23], a[116:119], v[120:123]\n\t"
    "s_mov_b32 s66, 131072\n\t"
    "v_mfma_f32_16x16x32_bf16 v[124:127], a[24:27], a[112:115], v[124:127]\n\t"
    "s_add_u32 m0, s64, 155648\n\t"
    "v_mfma_f32_16x16x32_bf16 v[124:127], a[28:31], a[116:119], v[124:127]\n\t"
    "buffer_load_dwordx4 v233, s[48:51], s66 offen lds\n\t"
    "v_mfma_f32_16x16x32_bf16 v[128:131], a[32:35], a[112:115], v[128:131]\n\t"
    "s_mov_b32 s66, 196608\n\t"
    "v_mfma_f32_16x16x32_bf16 v[128:131], a[36:39], a[116:119], v[128:131]\n\t"
    "s_add_u32 m0, s64, 159744\n\t"
    "v_mfma_f32_16x16x32_bf16 v[132:135], a[40:43], a[112:115], v[132:135]\n\t"
    "buffer_load_dwordx4 v233, s[48:51], s66 offen lds\n\t"
    "v_mfma_f32_16x16x32_bf16 v[132:135], a[44:47], a[116:119], v[132:135]\n\t"
    "s_mov_b32 s66, 262144\n\t"
    "v_mfma_f32_16x16x32_bf16 v[136:139], a[48:51], a[112:115], v[136:139]\n\t"
    "s_add_u32 m0, s64, 0\n\t"
    "v_mfma_f32_16x16x32_bf16 v[136:139], a[52:55], a[116:119], v[136:139]\n\t"
    "buffer_load_dwordx4 v246, s[48:51], s66 offen lds\n\t"
    "s_waitcnt lgkmcnt(0)\n\t"
    "v_mfma_f32_16x16x32_bf16 v[0:3], a[56:59], a[120:123], v[0:3]\n\t"
    "ds_read_b128 a[136:139], v224 offset:36864\n\t"
    "v_mfma_f32_16x16x32_bf16 v[0:3], a[60:63], a[124:127], v[0:3]\n\t"
    "ds_read_b128 a[140:143], v225 offset:36864\n\t"
    "v_mfma_f32_16x16x32_bf16 v[4:7], a[64:67], a[120:123], v[4:7]\n\t"
    "s_mov_b32 s66, 327680\n\t"
    "v_mfma_f32_16x16x32_bf16 v[4:7], a[68:71], a[124:127], v[4:7]\n\t"
    "s_add_u32 m0, s64, 4096\n\t"
    "v_mfma_f32_16x16x32_bf16 v[8:11], a[72:75], a[120:123], v[8:11]\n\t"
    "buffer_load_dwordx4 v246, s[48:51], s66 offen lds\n\t"
    "v_mfma_f32_16x16x32_bf16 v[8:11], a[76:79], a[124:127], v[8:11]\n\t"
    "s_mov_b32 s66, 393216\n\t"
    "v_mfma_f32_16x16x32_bf16 v[12:15], a[80:83], a[120:123], v[12:15]\n\t"
    "s_add_u32 m0, s64, 8192\n\t"
    "v_mfma_f32_16x16x32_bf16 v[12:15], a[84:87], a[124:127], v[12:15]\n\t"
    "buffer_load_dwordx4 v232, s[48:51], s66 offen lds\n\t"
    "v_mfma_f32_16x16x32_bf16 v[16:19], a[88:91], a[120:123], v[16:19]\n\t"
    "s_mov_b32 s66, 458752\n\t"
    "v_mfma_f32_16x16x32_bf16 v[16:19], a[92:95], a[124:127], v[16:19]\n\t"
    "s_add_u32 m0, s64, 12288\n\t"
    "v_mfma_f32_16x16x32_bf16 v[20:23], a[96:99], a[120:123], v[20:23]\n\t"
    "buffer_load_dwordx4 v232, s[48:51], s66 offen lds\n\t"
    "v_mfma_f32_16x16x32_bf16 v[20:23], a[100:103], a[124:127], v[20:23]\n\t"
    "s_mov_b32 s66, 262144\n\t"
    "v_mfma_f32_16x16x32_bf16 v[24:27], a[104:107], a[120:123], v[24:27]\n\t"
    "s_add_u32 m0, s64, 16384\n\t"
    "v_mfma_f32_16x16x32_bf16 v[24:27], a[108:111], a[124:127], v[24:27]\n\t"
    "buffer_load_dwordx4 v247, s[48:51], s66 offen lds\n\t"
    "v_mfma_f32_16x16x32_bf16 v[28:31], a[56:59], a[128:131], v[28:31]\n\t"
    "ds_read_b128 a[112:115], v224 offset:38912\n\t"
    "v_mfma_f32_16x16x32_bf16 v[28:31], a[60:63], a[132:135], v[28:31]\n\t"
    "ds_read_b128 a[116:119], v225 offset:38912\n\t"
    "v_mfma_f32_16x16x32_bf16 v[32:35], a[64:67], a[128:131], v[32:35]\n\t"
    "s_mov_b32 s66, 327680\n\t"
    "v_mfma_f32_16x16x32_bf16 v[32:35], a[68:71], a[132:135], v[32:35]\n\t"
    "s_add_u32 m0, s64, 20480\n\t"
    "v_mfma_f32_16x16x32_bf16 v[36:39], a[72:75], a[128:131], v[36:39]\n\t"
    "buffer_load_dwordx4 v247, s[48:51], s66 offen lds\n\t"
    "v_mfma_f32_16x16x32_bf16 v[36:39], a[76:79], a[132:135], v[36:39]\n\t"
    "s_mov_b32 s66, 393216\n\t"
    "v_mfma_f32_16x16x32_bf16 v[40:43], a[80:83], a[128:131], v[40:43]\n\t"
    "s_add_u32 m0, s64, 24576\n\t"
    "v_mfma_f32_16x16x32_bf16 v[40:43], a[84:87], a[132:135], v[40:43]\n\t"
    "buffer_load_dwordx4 v233, s[48:51], s66 offen lds\n\t"
    "v_mfma_f32_16x16x32_bf16 v[44:47], a[88:91], a[128:131], v[44:47]\n\t"
    "s_mov_b32 s66, 458752\n\t"
    "v_mfma_f32_16x16x32_bf16 v[44:47], a[92:95], a[132:135], v[44:47]\n\t"
    "s_add_u32 m0, s64, 28672\n\t"
    "v_mfma_f32_16x16x32_bf16 v[48:51], a[96:99], a[128:131], v[48:51]\n\t"
    "buffer_load_dwordx4 v233, s[48:51], s66 offen lds\n\t"
    "v_mfma_f32_16x16x32_bf16 v[48:51], a[100:103], a[132:135], v[48:51]\n\t"
    "s_mov_b32 s66, 524288\n\t"
    "v_mfma_f32_16x16x32_bf16 v[52:55], a[104:107], a[128:131], v[52:55]\n\t"
    "s_add_u32 m0, s64, 65536\n\t"
    "v_mfma_f32_16x16x32_bf16 v[52:55], a[108:111], a[132:135], v[52:55]\n\t"
    "buffer_load_dwordx4 v246, s[48:51], s66 offen lds\n\t"
    "s_waitcnt vmcnt(18) lgkmcnt(0)\n\t"
    "s_barrier\n\t"
    "v_mfma_f32_16x16x32_bf16 v[56:59], a[56:59], a[136:139], v[56:59]\n\t"
    "ds_read_b128 a[120:123], v224 offset:18432\n\t"
    "v_mfma_f32_16x16x32_bf16 v[56:59], a[60:63], a[140:143], v[56:59]\n\t"
    "ds_read_b128 a[124:127], v225 offset:18432\n\t"
    "v_pk_mul_f32 v[238:239], v[0:1], s[78:79] op_sel_hi:[1,0]\n\t"
    "v_pk_mul_f32 v[240:241], v[2:3], s[78:79] op_sel_hi:[1,0]\n\t"
    "v_pk_mul_f32 v[242:243], v[28:29], s[78:79] op_sel_hi:[1,0]\n\t"
    "v_pk_mul_f32 v[244:245], v[30:31], s[78:79] op_sel_hi:[1,0]\n\t"
    "v_mfma_f32_16x16x32_bf16 v[60:63], a[64:67], a[136:139], v[60:63]\n\t"
    "s_mov_b32 s66, 589824\n\t"
    "v_exp_f32 v238, v238\n\t"
    "v_mfma_f32_16x16x32_bf16 v[60:63], a[68:71], a[140:143], v[60:63]\n\t"
    "s_add_u32 m0, s64, 69632\n\t"
    "v_exp_f32 v239, v239\n\t"
    "v_mfma_f32_16x16x32_bf16 v[64:67], a[72:75], a[136:139], v[64:67]\n\t"
    "buffer_load_dwordx4 v246, s[48:51], s66 offen lds\n\t"
    "v_exp_f32 v240, v240\n\t"
    "v_mfma_f32_16x16x32_bf16 v[64:67], a[76:79], a[140:143], v[64:67]\n\t"
    "s_mov_b32 s66, 655360\n\t"
    "v_exp_f32 v241, v241\n\t"
    "v_mfma_f32_16x16x32_bf16 v[68:71], a[80:83], a[136:139], v[68:71]\n\t"
    "s_add_u32 m0, s64, 73728\n\t"
    "v_exp_f32 v242, v242\n\t"
    "v_mfma_f32_16x16x32_bf16 v[68:71], a[84:87], a[140:143], v[68:71]\n\t"
    "buffer_load_dwordx4 v232, s[48:51], s66 offen lds\n\t"
    "v_exp_f32 v243, v243\n\t"
    "v_exp_f32 v244, v244\n\t"
    "v_exp_f32 v245, v245\n\t"
    "v_pk_add_f32 v[238:239], v[238:239], 1.0 op_sel_hi:[1,0]\n\t"
    "v_pk_add_f32 v[240:241], v[240:241], 1.0 op_sel_hi:[1,0]\n\t"
    "v_pk_add_f32 v[242:243], v[242:243], 1.0 op_sel_hi:[1,0]\n\t"
    "v_pk_add_f32 v[244:245], v[244:245], 1.0 op_sel_hi:[1,0]\n\t"
    "v_mfma_f32_16x16x32_bf16 v[72:75], a[88:91], a[136:139], v[72:75]\n\t"
    "s_mov_b32 s66, 720896\n\t"
    "v_rcp_f32 v238, v238\n\t"
    "ST2_%=:\n\t"
    "s_cbranch_execz ST6_%=\n\t"
    "v_mfma_f32_16x16x32_bf16 v[72:75], a[92:95], a[140:143], v[72:75]\n\t"
    "s_add_u32 m0, s64, 77824\n\t"
    "v_rcp_f32 v239, v239\n\t"
    "v_mfma_f32_16x16x32_bf16 v[76:79], a[96:99], a[136:139], v[76:79]\n\t"
    "buffer_load_dwordx4 v232, s[48:51], s66 offen lds\n\t"
    "v_rcp_f32 v240, v240\n\t"
    "v_mfma_f32_16x16x32_bf16 v[76:79], a[100:103], a[140:143], v[76:79]\n\t"
    "s_mov_b32 s66, 524288\n\t"
    "v_rcp_f32 v241, v241\n\t"
    "v_mfma_f32_16x16x32_bf16 v[80:83], a[104:107], a[136:139], v[80:83]\n\t"
    "s_add_u32 m0, s64, 32768\n\t"
    "v_rcp_f32 v242, v242\n\t"
    "v_mfma_f32_16x16x32_bf16 v[80:83], a[108:111], a[140:143], v[80:83]\n\t"
    "buffer_load_dwordx4 v247, s[48:51], s66 offen lds\n\t"
    "v_rcp_f32 v243, v243\n\t"
    "v_rcp_f32 v244, v244\n\t"
    "v_rcp_f32 v245, v245\n\t"
    "v_pk_mul_f32 v[0:1], v[0:1], v[12:13]\n\t"
    "v_pk_mul_f32 v[2:3], v[2:3], v[14:15]\n\t"
    "v_pk_mul_f32 v[28:29], v[28:29], v[40:41]\n\t"
    "v_pk_mul_f32 v[30:31], v[30:31], v[42:43]\n\t"
    "v_pk_mul_f32 v[0:1], v[0:1], v[238:239]\n\t"
    "v_pk_mul_f32 v[2:3], v[2:3], v[240:241]\n\t"
    "v_pk_mul_f32 v[28:29], v[28:29], v[242:243]\n\t"
    "v_pk_mul_f32 v[30:31], v[30:31], v[244:245]\n\t"
    "v_cvt_pk_bf16_f32 v12, v0, v1\n\t"
    "v_cvt_pk_bf16_f32 v14, v28, v29\n\t"
    "v_cvt_pk_bf16_f32 v13, v2, v3\n\t"
    "v_cvt_pk_bf16_f32 v15, v30, v31\n\t"
    "v_pk_mul_f32 v[238:239], v[4:5], s[78:79] op_sel_hi:[1,0]\n\t"
    "v_pk_mul_f32 v[240:241], v[6:7], s[78:79] op_sel_hi:[1,0]\n\t"
    "v_pk_mul_f32 v[242:243], v[32:33], s[78:79] op_sel_hi:[1,0]\n\t"
    "v_pk_mul_f32 v[244:245], v[34:35], s[78:79] op_sel_hi:[1,0]\n\t"
    "v_permlane16_swap_b32 v12, v14\n\t"
    "v_permlane16_swap_b32 v13, v15\n\t"
    "s_mov_b32 s67, 0\n\t"
    "buffer_store_dwordx4 v[12:15], v236, s[52:55], s67 offen offset:0 sc1\n\t"
    "v_mfma_f32_16x16x32_bf16 v[84:87], a[56:59], a[112:115], v[84:87]\n\t"
    "ds_read_b128 a[128:131], v224 offset:49152\n\t"
    "v_exp_f32 v238, v238\n\t"
    "v_mfma_f32_16x16x32_bf16 v[84:87], a[60:63], a[116:119], v[84:87]\n\t"
    "ds_read_b128 a[132:135], v225 offset:49152\n\t"
    "v_exp_f32 v239, v239\n\t"
    "v_mfma_f32_16x16x32_bf16 v[88:91], a[64:67], a[112:115], v[88:91]\n\t"
    "s_mov_b32 s66, 589824\n\t"
    "v_exp_f32 v240, v240\n\t"
    "v_mfma_f32_16x16x32_bf16 v[88:91], a[68:71], a[116:119], v[88:91]\n\t"
    "s_add_u32 m0, s64, 36864\n\t"
    "v_exp_f32 v241, v241\n\t"
    "v_mfma_f32_16x16x32_bf16 v[92:95], a[72:75], a[112:115], v[92:95]\n\t"
    "buffer_load_dwordx4 v247, s[48:51], s66 offen lds\n\t"
    "v_exp_f32 v242, v242\n\t"
    "v_mfma_f32_16x16x32_bf16 v[92:95], a[76:79], a[116:119], v[92:95]\n\t"
    "s_mov_b32 s66, 655360\n\t"
    "v_exp_f32 v243, v243\n\t"
    "v_exp_f32 v244, v244\n\t"
    "v_exp_f32 v245, v245\n\t"
    "v_pk_add_f32 v[238:239], v[238:239], 1.0 op_sel_hi:[1,0]\n\t"
    "v_pk_add_f32 v[240:241], v[240:241], 1.0 op_sel_hi:[1,0]\n\t"
    "v_pk_add_f32 v[242:243], v[242:243], 1.0 op_sel_hi:[1,0]\n\t"
    "v_pk_add_f32 v[244:245], v[244:245], 1.0 op_sel_hi:[1,0]\n\t"
    "v_mfma_f32_16x16x32_bf16 v[96:99], a[80:83], a[112:115], v[96:99]\n\t"
    "s_add_u32 m0, s64, 40960\n\t"
    "v_rcp_f32 v238, v238\n\t"
    "v_mfma_f32_16x16x32_bf16 v[96:99], a[84:87], a[116:119], v[96:99]\n\t"
    "buffer_load_dwordx4 v233, s[48:51], s66 offen lds\n\t"
    "v_rcp_f32 v239, v239\n\t"
    "v_mfma_f32_16x16x32_bf16 v[100:103], a[88:91], a[112:115], v[100:103]\n\t"
    "s_mov_b32 s66, 720896\n\t"
    "v_rcp_f32 v240, v240\n\t"
    "v_mfma_f32_16x16x32_bf16 v[100:103], a[92:95], a[116:119], v[100:103]\n\t"
    "s_add_u32 m0, s64, 45056\n\t"
    "v_rcp_f32 v241, v241\n\t"
    "v_mfma_f32_16x16x32_bf16 v[104:107], a[96:99], a[112:115], v[104:107]\n\t"
    "buffer_load_dwordx4 v233, s[48:51], s66 offen lds\n\t"
    "v_rcp_f32 v242, v242\n\t"
    "v_mfma_f32_16x16x32_bf16 v[104:107], a[100:103], a[116:119], v[104:107]\n\t"
    "s_mov_b32 s66, 786432\n\t"
    "v_rcp_f32 v243, v243\n\t"
    "v_rcp_f32 v244, v244\n\t"
    "v_rcp_f32 v245, v245\n\t"
    "v_pk_mul_f32 v[4:5], v[4:5], v[16:17]\n\t"
    "v_pk_mul_f32 v[6:7], v[6:7], v[18:19]\n\t"
    "v_pk_mul_f32 v[32:33], v[32:33], v[44:45]\n\t"
    "v_pk_mul_f32 v[34:35], v[34:35], v[46:47]\n\t"
    "v_pk_mul_f32 v[4:5], v[4:5], v[238:239]\n\t"
    "v_pk_mul_f32 v[6:7], v[6:7], v[240:241]\n\t"
    "v_pk_mul_f32 v[32:33], v[32:33], v[242:243]\n\t"
    "v_pk_mul_f32 v[34:35], v[34:35], v[244:245]\n\t"
    "v_cvt_pk_bf16_f32 v16, v4, v5\n\t"
    "v_cvt_pk_bf16_f32 v18, v32, v33\n\t"
    "v_cvt_pk_bf16_f32 v17, v6, v7\n\t"
    "v_cvt_pk_bf16_f32 v19, v34, v35\n\t"
    "v_pk_mul_f32 v[238:239], v[8:9], s[78:79] op_sel_hi:[1,0]\n\t"
    "v_pk_mul_f32 v[240:241], v[10:11], s[78:79] op_sel_hi:[1,0]\n\t"
    "v_pk_mul_f32 v[242:243], v[36:37], s[78:79] op_sel_hi:[1,0]\n\t"
    "v_pk_mul_f32 v[244:245], v[38:39], s[78:79] op_sel_hi:[1,0]\n\t"
    "v_permlane16_swap_b32 v16, v18\n\t"
    "v_permlane16_swap_b32 v17, v19\n\t"
    "s_mov_b32 s67, 0\n\t"
    "buffer_store_dwordx4 v[16:19], v236, s[52:55], s67 offen offset:512 sc1\n\t"
    "v_mfma_f32_16x16x32_bf16 v[108:111], a[104:107], a[112:115], v[108:111]\n\t"
    "s_add_u32 m0, s64, 49152\n\t"
    "v_exp_f32 v238, v238\n\t"
    "v_mfma_f32_16x16x32_bf16 v[108:111], a[108:111], a[116:119], v[108:111]\n\t"
    "buffer_load_dwordx4 v246, s[48:51], s66 offen lds\n\t"
    "v_exp_f32 v239, v239\n\t"
    "s_waitcnt lgkmcnt(2)\n\t"
    "v_mfma_f32_16x16x32_bf16 v[140:143], a[0:3], a[120:123], v[140:143]\n\t"
    "ds_read_b128 a[136:139], v224 offset:51200\n\t"
    "v_exp_f32 v240, v240\n\t"
    "v_mfma_f32_16x16x32_bf16 v[140:143], a[4:7], a[124:127], v[140:143]\n\t"
    "ds_read_b128 a[140:143], v225 offset:51200\n\t"
    "v_exp_f32 v241, v241\n\t"
    "v_mfma_f32_16x16x32_bf16 v[144:147], a[8:11], a[120:123], v[144:147]\n\t"
    "s_mov_b32 s66, 851968\n\t"
    "v_exp_f32 v242, v242\n\t"
    "v_mfma_f32_16x16x32_bf16 v[144:147], a[12:15], a[124:127], v[144:147]\n\t"
    "s_add_u32 m0, s64, 53248\n\t"
    "v_exp_f32 v243, v243\n\t"
    "v_exp_f32 v244, v244\n\t"
    "v_exp_f32 v245, v245\n\t"
    "v_pk_add_f32 v[238:239], v[238:239], 1.0 op_sel_hi:[1,0]\n\t"
    "v_pk_add_f32 v[240:241], v[240:241], 1.0 op_sel_hi:[1,0]\n\t"
    "v_pk_add_f32 v[242:243], v[242:243], 1.0 op_sel_hi:[1,0]\n\t"
    "v_pk_add_f32 v[244:245], v[244:245], 1.0 op_sel_hi:[1,0]\n\t"
    "v_mfma_f32_16x16x32_bf16 v[148:151], a[16:19], a[120:123], v[148:151]\n\t"
    "buffer_load_dwordx4 v246, s[48:51], s66 offen lds\n\t"
    "v_rcp_f32 v238, v238\n\t"
    "v_mfma_f32_16x16x32_bf16 v[148:151], a[20:23], a[124:127], v[148:151]\n\t"
    "s_mov_b32 s66, 917504\n\t"
    "v_rcp_f32 v239, v239\n\t"
    "v_mfma_f32_16x16x32_bf16 v[152:155], a[24:27], a[120:123], v[152:155]\n\t"
    "s_add_u32 m0, s64, 57344\n\t"
    "v_rcp_f32 v240, v240\n\t"
    "v_mfma_f32_16x16x32_bf16 v[152:155], a[28:31], a[124:127], v[152:155]\n\t"
    "buffer_load_dwordx4 v232, s[48:51], s66 offen lds\n\t"
    "v_rcp_f32 v241, v241\n\t"
    "v_mfma_f32_16x16x32_bf16 v[156:159], a[32:35], a[120:123], v[156:159]\n\t"
    "s_mov_b32 s66, 983040\n\t"
    "v_rcp_f32 v242, v242\n\t"
    "v_mfma_f32_16x16x32_bf16 v[156:159], a[36:39], a[124:127], v[156:159]\n\t"
    "s_add_u32 m0, s64, 61440\n\t"
    "v_rcp_f32 v243, v243\n\t"
    "v_rcp_f32 v244, v244\n\t"
    "v_rcp_f32 v245, v245\n\t"
    "v_pk_mul_f32 v[8:9], v[8:9], v[20:21]\n\t"
    "v_pk_mul_f32 v[10:11], v[10:11], v[22:23]\n\t"
    "v_pk_mul_f32 v[36:37], v[36:37], v[48:49]\n\t"
    "v_pk_mul_f32 v[38:39], v[38:39], v[50:51]\n\t"
    "v_pk_mul_f32 v[8:9], v[8:9], v[238:239]\n\t"
    "v_pk_mul_f32 v[10:11], v[10:11], v[240:241]\n\t"
    "v_pk_mul_f32 v[36:37], v[36:37], v[242:243]\n\t"
    "v_pk_mul_f32 v[38:39], v[38:39], v[244:245]\n\t"
    "v_cvt_pk_bf16_f32 v20, v8, v9\n\t"
    "v_cvt_pk_bf16_f32 v22, v36, v37\n\t"
    "v_cvt_pk_bf16_f32 v21, v10, v11\n\t"
    "v_cvt_pk_bf16_f32 v23, v38, v39\n\t"
    "s_nop 1\n\t"
    "v_permlane16_swap_b32 v20, v22\n\t"
    "v_permlane16_swap_b32 v21, v23\n\t"
    "s_mov_b32 s67, 0\n\t"
    "buffer_store_dwordx4 v[20:23], v236, s[52:55], s67 offen offset:1024 sc1\n\t"
    "v_pk_mul_f32 v[238:239], v[56:57], s[78:79] op_sel_hi:[1,0]\n\t"
    "v_pk_mul_f32 v[240:241], v[58:59], s[78:79] op_sel_hi:[1,0]\n\t"
    "v_pk_mul_f32 v[242:243], v[84:85], s[78:79] op_sel_hi:[1,0]\n\t"
    "v_pk_mul_f32 v[244:245], v[86:87], s[78:79] op_sel_hi:[1,0]\n\t"
    "v_mfma_f32_16x16x32_bf16 v[160:163], a[40:43], a[120:123], v[160:163]\n\t"
    "buffer_load_dwordx4 v232, s[48:51], s66 offen lds\n\t"
    "v_exp_f32 v238, v238\n\t"
    "v_mfma_f32_16x16x32_bf16 v[160:163], a[44:47], a[124:127], v[160:163]\n\t"
    "s_mov_b32 s66, 786432\n\t"
    "v_exp_f32 v239, v239\n\t"
    "v_mfma_f32_16x16x32_bf16 v[164:167], a[48:51], a[120:123], v[164:167]\n\t"
    "s_add_u32 m0, s64, 98304\n\t"
    "v_exp_f32 v240, v240\n\t"
    "v_mfma_f32_16x16x32_bf16 v[164:167], a[52:55], a[124:127], v[164:167]\n\t"
    "buffer_load_dwordx4 v247, s[48:51], s66 offen lds\n\t"
    "v_exp_f32 v241, v241\n\t"
    "s_waitcnt lgkmcnt(2)\n\t"
    "v_mfma_f32_16x16x32_bf16 v[112:115], a[56:59], a[128:131], v[112:115]\n\t"
    "ds_read_b128 a[112:115], v224 offset:20480\n\t"
    "v_exp_f32 v242, v242\n\t"
    "v_mfma_f32_16x16x32_bf16 v[112:115], a[60:63], a[132:135], v[112:115]\n\t"
    "ds_read_b128 a[116:119], v225 offset:20480\n\t"
    "v_exp_f32 v243, v243\n\t"
    "v_exp_f32 v244, v244\n\t"
    "v_exp_f32 v245, v245\n\t"
    "v_pk_add_f32 v[238:239], v[238:239], 1.0 op_sel_hi:[1,0]\n\t"
    "v_pk_add_f32 v[240:241], v[240:241], 1.0 op_sel_hi:[1,0]\n\t"
    "v_pk_add_f32 v[242:243], v[242:243], 1.0 op_sel_hi:[1,0]\n\t"
    "v_pk_add_f32 v[244:245], v[244:245], 1.0 op_sel_hi:[1,0]\n\t"
    "v_mfma_f32_16x16x32_bf16 v[116:119], a[64:67], a[128:131], v[116:119]\n\t"
    "s_mov_b32 s66, 851968\n\t"
    "v_rcp_f32 v238, v238\n\t"
    "v_mfma_f32_16x16x32_bf16 v[116:119], a[68:71], a[132:135], v[116:119]\n\t"
    "s_add_u32 m0, s64, 102400\n\t"
    "v_rcp_f32 v239, v239\n\t"
    "v_mfma_f32_16x16x32_bf16 v[120:123], a[72:75], a[128:131], v[120:123]\n\t"
    "buffer_load_dwordx4 v247, s[48:51], s66 offen lds\n\t"
    "v_rcp_f32 v240, v240\n\t"
    "v_mfma_f32_16x16x32_bf16 v[120:123], a[76:79], a[132:135], v[120:123]\n\t"
    "s_mov_b32 s66, 917504\n\t"
    "v_rcp_f32 v241, v241\n\t"
    "v_mfma_f32_16x16x32_bf16 v[124:127], a[80:83], a[128:131], v[124:127]\n\t"
    "s_add_u32 m0, s64, 106496\n\t"
    "v_rcp_f32 v242, v242\n\t"
    "v_mfma_f32_16x16x32_bf16 v[124:127], a[84:87], a[132:135], v[124:127]\n\t"
    "buffer_load_dwordx4 v233, s[48:51], s66 offen lds\n\t"
    "v_rcp_f32 v243, v243\n\t"
    "v_rcp_f32 v244, v244\n\t"
    "v_rcp_f32 v245, v245\n\t"
    "v_pk_mul_f32 v[56:57], v[56:57], v[68:69]\n\t"
    "v_pk_mul_f32 v[58:59], v[58:59], v[70:71]\n\t"
    "v_pk_mul_f32 v[84:85], v[84:85], v[96:97]\n\t"
    "v_pk_mul_f32 v[86:87], v[86:87], v[98:99]\n\t"
    "v_pk_mul_f32 v[56:57], v[56:57], v[238:239]\n\t"
    "v_pk_mul_f32 v[58:59], v[58:59], v[240:241]\n\t"
    "v_pk_mul_f32 v[84:85], v[84:85], v[242:243]\n\t"
    "v_pk_mul_f32 v[86:87], v[86:87], v[244:245]\n\t"
    "v_cvt_pk_bf16_f32 v68, v56, v57\n\t"
    "v_cvt_pk_bf16_f32 v70, v84, v85\n\t"
    "v_cvt_pk_bf16_f32 v69, v58, v59\n\t"
    "v_cvt_pk_bf16_f32 v71, v86, v87\n\t"
    "v_pk_mul_f32 v[238:239], v[60:61], s[78:79] op_sel_hi:[1,0]\n\t"
    "v_pk_mul_f32 v[240:241], v[62:63], s[78:79] op_sel_hi:[1,0]\n\t"
    "v_pk_mul_f32 v[242:243], v[88:89], s[78:79] op_sel_hi:[1,0]\n\t"
    "v_pk_mul_f32 v[244:245], v[90:91], s[78:79] op_sel_hi:[1,0]\n\t"
    "v_permlane16_swap_b32 v68, v70\n\t"
    "v_permlane16_swap_b32 v69, v71\n\t"
    "s_mov_b32 s67, 229376\n\t"
    "buffer_store_dwordx4 v[68:71], v236, s[52:55], s67 offen offset:0 sc1\n\t"
    "v_mfma_f32_16x16x32_bf16 v[128:131], a[88:91], a[128:131], v[128:131]\n\t"
    "s_mov_b32 s66, 983040\n\t"
    "v_exp_f32 v238, v238\n\t"
    "v_mfma_f32_16x16x32_bf16 v[128:131], a[92:95], a[132:135], v[128:131]\n\t"
    "s_add_u32 m0, s64, 110592\n\t"
    "v_exp_f32 v239, v239\n\t"
    "v_mfma_f32_16x16x32_bf16 v[132:135], a[96:99], a[128:131], v[132:135]\n\t"
    "buffer_load_dwordx4 v233, s[48:51], s66 offen lds\n\t"
    "v_exp_f32 v240, v240\n\t"
    "v_mfma_f32_16x16x32_bf16 v[132:135], a[100:103], a[132:135], v[132:135]\n\t"
    "v_exp_f32 v241, v241\n\t"
    "v_mfma_f32_16x16x32_bf16 v[136:139], a[104:107], a[128:131], v[136:139]\n\t"
    "v_exp_f32 v242, v242\n\t"
    "v_mfma_f32_16x16x32_bf16 v[136:139], a[108:111], a[132:135], v[136:139]\n\t"
    "v_exp_f32 v243, v243\n\t"
    "v_exp_f32 v244, v244\n\t"
    "v_exp_f32 v245, v245\n\t"
    "v_pk_add_f32 v[238:239], v[238:239], 1.0 op_sel_hi:[1,0]\n\t"
    "v_pk_add_f32 v[240:241], v[240:241], 1.0 op_sel_hi:[1,0]\n\t"
    "v_pk_add_f32 v[242:243], v[242:243], 1.0 op_sel_hi:[1,0]\n\t"
    "v_pk_add_f32 v[244:245], v[244:245], 1.0 op_sel_hi:[1,0]\n\t"
    "s_waitcnt lgkmcnt(2)\n\t"
    "v_mfma_f32_16x16x32_bf16 v[140:143], a[56:59], a[136:139], v[140:143]\n\t"
    "ds_read_b128 a[120:123], v224 offset:22528\n\t"
    "v_rcp_f32 v238, v238\n\t"
    "v_mfma_f32_16x16x32_bf16 v[140:143], a[60:63], a[140:143], v[140:143]\n\t"
    "ds_read_b128 a[124:127], v225 offset:22528\n\t"
    "v_rcp_f32 v239, v239\n\t"
    "v_mfma_f32_16x16x32_bf16 v[144:147], a[64:67], a[136:139], v[144:147]\n\t"
    "v_rcp_f32 v240, v240\n\t"
    "v_mfma_f32_16x16x32_bf16 v[144:147], a[68:71], a[140:143], v[144:147]\n\t"
    "v_rcp_f32 v241, v241\n\t"
    "v_mfma_f32_16x16x32_bf16 v[148:151], a[72:75], a[136:139], v[148:151]\n\t"
    "v_rcp_f32 v242, v242\n\t"
    "v_mfma_f32_16x16x32_bf16 v[148:151], a[76:79], a[140:143], v[148:151]\n\t"
    "v_rcp_f32 v243, v243\n\t"
    "v_rcp_f32 v244, v244\n\t"
    "v_rcp_f32 v245, v245\n\t"
    "v_pk_mul_f32 v[60:61], v[60:61], v[72:73]\n\t"
    "v_pk_mul_f32 v[62:63], v[62:63], v[74:75]\n\t"
    "v_pk_mul_f32 v[88:89], v[88:89], v[100:101]\n\t"
    "v_pk_mul_f32 v[90:91], v[90:91], v[102:103]\n\t"
    "v_pk_mul_f32 v[60:61], v[60:61], v[238:239]\n\t"
    "v_pk_mul_f32 v[62:63], v[62:63], v[240:241]\n\t"
    "v_pk_mul_f32 v[88:89], v[88:89], v[242:243]\n\t"
    "v_pk_mul_f32 v[90:91], v[90:91], v[244:245]\n\t"
    "v_cvt_pk_bf16_f32 v72, v60, v61\n\t"
    "v_cvt_pk_bf16_f32 v74, v88, v89\n\t"
    "v_cvt_pk_bf16_f32 v73, v62, v63\n\t"
    "v_cvt_pk_bf16_f32 v75, v90, v91\n\t"
    "v_pk_mul_f32 v[238:239], v[64:65], s[78:79] op_sel_hi:[1,0]\n\t"
    "v_pk_mul_f32 v[240:241], v[66:67], s[78:79] op_sel_hi:[1,0]\n\t"
    "v_pk_mul_f32 v[242:243], v[92:93], s[78:79] op_sel_hi:[1,0]\n\t"
    "v_pk_mul_f32 v[244:245], v[94:95], s[78:79] op_sel_hi:[1,0]\n\t"
    "v_permlane16_swap_b32 v72, v74\n\t"
    "v_permlane16_swap_b32 v73, v75\n\t"
    "s_mov_b32 s67, 229376\n\t"
    "buffer_store_dwordx4 v[72:75], v236, s[52:55], s67 offen offset:512 sc1\n\t"
    "v_mfma_f32_16x16x32_bf16 v[152:155], a[80:83], a[136:139], v[152:155]\n\t"
    "v_exp_f32 v238, v238\n\t"
    "v_mfma_f32_16x16x32_bf16 v[152:155], a[84:87], a[140:143], v[152:155]\n\t"
    "v_exp_f32 v239, v239\n\t"
    "v_mfma_f32_16x16x32_bf16 v[156:159], a[88:91], a[136:139], v[156:159]\n\t"
    "v_exp_f32 v240, v240\n\t"
    "v_mfma_f32_16x16x32_bf16 v[156:159], a[92:95], a[140:143], v[156:159]\n\t"
    "v_exp_f32 v241, v241\n\t"
    "v_mfma_f32_16x16x32_bf16 v[160:163], a[96:99], a[136:139], v[160:163]\n\t"
    "v_exp_f32 v242, v242\n\t"
    "v_mfma_f32_16x16x32_bf16 v[160:163], a[100:103], a[140:143], v[160:163]\n\t"
    "v_exp_f32 v243, v243\n\t"
    "v_exp_f32 v244, v244\n\t"
    "v_exp_f32 v245, v245\n\t"
    "v_pk_add_f32 v[238:239], v[238:239], 1.0 op_sel_hi:[1,0]\n\t"
    "v_pk_add_f32 v[240:241], v[240:241], 1.0 op_sel_hi:[1,0]\n\t"
    "v_pk_add_f32 v[242:243], v[242:243], 1.0 op_sel_hi:[1,0]\n\t"
    "v_pk_add_f32 v[244:245], v[244:245], 1.0 op_sel_hi:[1,0]\n\t"
    "v_mfma_f32_16x16x32_bf16 v[164:167], a[104:107], a[136:139], v[164:167]\n\t"
    "v_rcp_f32 v238, v238\n\t"
    "v_mfma_f32_16x16x32_bf16 v[164:167], a[108:111], a[140:143], v[164:167]\n\t"
    "v_rcp_f32 v239, v239\n\t"
    "v_rcp_f32 v240, v240\n\t"
    "v_rcp_f32 v241, v241\n\t"
    "v_rcp_f32 v242, v242\n\t"
    "v_rcp_f32 v243, v243\n\t"
    "v_rcp_f32 v244, v244\n\t"
    "v_rcp_f32 v245, v245\n\t"
    "v_pk_mul_f32 v[64:65], v[64:65], v[76:77]\n\t"
    "v_pk_mul_f32 v[66:67], v[66:67], v[78:79]\n\t"
    "v_pk_mul_f32 v[92:93], v[92:93], v[104:105]\n\t"
    "v_pk_mul_f32 v[94:95], v[94:95], v[106:107]\n\t"
    "v_pk_mul_f32 v[64:65], v[64:65], v[238:239]\n\t"
    "v_pk_mul_f32 v[66:67], v[66:67], v[240:241]\n\t"
    "v_pk_mul_f32 v[92:93], v[92:93], v[242:243]\n\t"
    "v_pk_mul_f32 v[94:95], v[94:95], v[244:245]\n\t"
    "v_cvt_pk_bf16_f32 v76, v64, v65\n\t"
    "v_cvt_pk_bf16_f32 v78, v92, v93\n\t"
    "v_cvt_pk_bf16_f32 v77, v66, v67\n\t"
    "v_cvt_pk_bf16_f32 v79, v94, v95\n\t"
    "v_permlane32_swap_b32 v24, v52\n\t"
    "v_permlane32_swap_b32 v25, v53\n\t"
    "v_permlane32_swap_b32 v26, v54\n\t"
    "v_permlane32_swap_b32 v27, v55\n\t"
    "v_permlane32_swap_b32 v80, v108\n\t"
    "v_permlane32_swap_b32 v81, v109\n\t"
    "v_permlane32_swap_b32 v82, v110\n\t"
    "v_permlane32_swap_b32 v83, v111\n\t"
    "v_pk_mul_f32 v[238:239], v[24:25], s[78:79] op_sel_hi:[1,0]\n\t"
    "v_pk_mul_f32 v[240:241], v[26:27], s[78:79] op_sel_hi:[1,0]\n\t"
    "v_pk_mul_f32 v[242:243], v[80:81], s[78:79] op_sel_hi:[1,0]\n\t"
    "v_pk_mul_f32 v[244:245], v[82:83], s[78:79] op_sel_hi:[1,0]\n\t"
    "v_permlane16_swap_b32 v76, v78\n\t"
    "v_permlane16_swap_b32 v77, v79\n\t"
    "s_mov_b32 s67, 229376\n\t"
    "buffer_store_dwordx4 v[76:79], v236, s[52:55], s67 offen offset:1024 sc1\n\t"
    "v_exp_f32 v238, v238\n\t"
    "v_exp_f32 v239, v239\n\t"
    "v_exp_f32 v240, v240\n\t"
    "v_exp_f32 v241, v241\n\t"
    "v_exp_f32 v242, v242\n\t"
    "v_exp_f32 v243, v243\n\t"
    "v_exp_f32 v244, v244\n\t"
    "v_exp_f32 v245, v245\n\t"
    "v_pk_add_f32 v[238:239], v[238:239], 1.0 op_sel_hi:[1,0]\n\t"
    "v_pk_add_f32 v[240:241], v[240:241], 1.0 op_sel_hi:[1,0]\n\t"
    "v_pk_add_f32 v[242:243], v[242:243], 1.0 op_sel_hi:[1,0]\n\t"
    "v_pk_add_f32 v[244:245], v[244:245], 1.0 op_sel_hi:[1,0]\n\t"
    "v_rcp_f32 v238, v238\n\t"
    "v_rcp_f32 v239, v239\n\t"
    "v_rcp_f32 v240, v240\n\t"
    "v_rcp_f32 v241, v241\n\t"
    "v_rcp_f32 v242, v242\n\t"
    "v_rcp_f32 v243, v243\n\t"
    "v_rcp_f32 v244, v244\n\t"
    "v_rcp_f32 v245, v245\n\t"
    "v_pk_mul_f32 v[24:25], v[24:25], v[52:53]\n\t"
    "v_pk_mul_f32 v[26:27], v[26:27], v[54:55]\n\t"
    "v_pk_mul_f32 v[80:81], v[80:81], v[108:109]\n\t"
    "v_pk_mul_f32 v[82:83], v[82:83], v[110:111]\n\t"
    "v_pk_mul_f32 v[24:25], v[24:25], v[238:239]\n\t"
    "v_pk_mul_f32 v[26:27], v[26:27], v[240:241]\n\t"
    "v_pk_mul_f32 v[80:81], v[80:81], v[242:243]\n\t"
    "v_pk_mul_f32 v[82:83], v[82:83], v[244:245]\n\t"
    "v_cvt_pk_bf16_f32 v52, v24, v25\n\t"
    "v_cvt_pk_bf16_f32 v54, v80, v81\n\t"
    "v_cvt_pk_bf16_f32 v53, v26, v27\n\t"
    "v_cvt_pk_bf16_f32 v55, v82, v83\n\t"
    "s_nop 1\n\t"
    "v_permlane16_swap_b32 v52, v54\n\t"
    "v_permlane16_swap_b32 v53, v55\n\t"
    "s_mov_b32 s67, 0\n\t"
    "buffer_store_dwordx4 v[52:55], v237, s[52:55], s67 offen sc1\n\t"
    "s_waitcnt lgkmcnt(2)\n\t"
    "v_mfma_f32_16x16x32_bf16 v[168:171], a[0:3], a[112:115], v[168:171]\n\t"
    "ds_read_b128 a[128:131], v224 offset:53248\n\t"
    "v_mfma_f32_16x16x32_bf16 v[168:171], a[4:7], a[116:119], v[168:171]\n\t"
    "ds_read_b128 a[132:135], v225 offset:53248\n\t"
    "v_pk_mul_f32 v[238:239], v[112:113], s[78:79] op_sel_hi:[1,0]\n\t"
    "v_pk_mul_f32 v[240:241], v[114:115], s[78:79] op_sel_hi:[1,0]\n\t"
    "v_pk_mul_f32 v[242:243], v[140:141], s[78:79] op_sel_hi:[1,0]\n\t"
    "v_pk_mul_f32 v[244:245], v[142:143], s[78:79] op_sel_hi:[1,0]\n\t"
    "v_mfma_f32_16x16x32_bf16 v[172:175], a[8:11], a[112:115], v[172:175]\n\t"
    "v_exp_f32 v238, v238\n\t"
    "v_mfma_f32_16x16x32_bf16 v[172:175], a[12:15], a[116:119], v[172:175]\n\t"
    "v_exp_f32 v239, v239\n\t"
    "v_mfma_f32_16x16x32_bf16 v[176:179], a[16:19], a[112:115], v[176:179]\n\t"
    "v_exp_f32 v240, v240\n\t"
    "v_mfma_f32_16x16x32_bf16 v[176:179], a[20:23], a[116:119], v[176:179]\n\t"
    "v_exp_f32 v241, v241\n\t"
    "v_mfma_f32_16x16x32_bf16 v[180:183], a[24:27], a[112:115], v[180:183]\n\t"
    "v_exp_f32 v242, v242\n\t"
    "v_mfma_f32_16x16x32_bf16 v[180:183], a[28:31], a[116:119], v[180:183]\n\t"
    "v_exp_f32 v243, v243\n\t"
    "v_exp_f32 v244, v244\n\t"
    "v_exp_f32 v245, v245\n\t"
    "v_pk_add_f32 v[238:239], v[238:239], 1.0 op_sel_hi:[1,0]\n\t"
    "v_pk_add_f32 v[240:241], v[240:241], 1.0 op_sel_hi:[1,0]\n\t"
    "v_pk_add_f32 v[242:243], v[242:243], 1.0 op_sel_hi:[1,0]\n\t"
    "v_pk_add_f32 v[244:245], v[244:245], 1.0 op_sel_hi:[1,0]\n\t"
    "v_mfma_f32_16x16x32_bf16 v[184:187], a[32:35], a[112:115], v[184:187]\n\t"
    "v_rcp_f32 v238, v238\n\t"
    "v_mfma_f32_16x16x32_bf16 v[184:187], a[36:39], a[116:119], v[184:187]\n\t"
    "v_rcp_f32 v239, v239\n\t"
    "v_mfma_f32_16x16x32_bf16 v[188:191], a[40:43], a[112:115], v[188:191]\n\t"
    "v_rcp_f32 v240, v240\n\t"
    "v_mfma_f32_16x16x32_bf16 v[188:191], a[44:47], a[116:119], v[188:191]\n\t"
    "v_rcp_f32 v241, v241\n\t"
    "v_mfma_f32_16x16x32_bf16 v[192:195], a[48:51], a[112:115], v[192:195]\n\t"
    "v_rcp_f32 v242, v242\n\t"
    "v_mfma_f32_16x16x32_bf16 v[192:195], a[52:55], a[116:119], v[192:195]\n\t"
    "v_rcp_f32 v243, v243\n\t"
    "v_rcp_f32 v244, v244\n\t"
    "v_rcp_f32 v245, v245\n\t"
    "v_pk_mul_f32 v[112:113], v[112:113], v[124:125]\n\t"
    "v_pk_mul_f32 v[114:115], v[114:115], v[126:127]\n\t"
    "v_pk_mul_f32 v[140:141], v[140:141], v[152:153]\n\t"
    "v_pk_mul_f32 v[142:143], v[142:143], v[154:155]\n\t"
    "v_pk_mul_f32 v[112:113], v[112:113], v[238:239]\n\t"
    "v_pk_mul_f32 v[114:115], v[114:115], v[240:241]\n\t"
    "v_pk_mul_f32 v[140:141], v[140:141], v[242:243]\n\t"
    "v_pk_mul_f32 v[142:143], v[142:143], v[244:245]\n\t"
    "v_cvt_pk_bf16_f32 v124, v112, v113\n\t"
    "v_cvt_pk_bf16_f32 v126, v140, v141\n\t"
    "v_cvt_pk_bf16_f32 v125, v114, v115\n\t"
    "v_cvt_pk_bf16_f32 v127, v142, v143\n\t"
    "v_pk_mul_f32 v[238:239], v[116:117], s[78:79] op_sel_hi:[1,0]\n\t"
    "v_pk_mul_f32 v[240:241], v[118:119], s[78:79] op_sel_hi:[1,0]\n\t"
    "v_pk_mul_f32 v[242:243], v[144:145], s[78:79] op_sel_hi:[1,0]\n\t"
    "v_pk_mul_f32 v[244:245], v[146:147], s[78:79] op_sel_hi:[1,0]\n\t"
    "v_permlane16_swap_b32 v124, v126\n\t"
    "v_permlane16_swap_b32 v125, v127\n\t"
    "s_mov_b32 s67, 458752\n\t"
    "buffer_store_dwordx4 v[124:127], v236, s[52:55], s67 offen offset:0 sc1\n\t"
    "s_waitcnt lgkmcnt(2)\n\t"
    "v_mfma_f32_16x16x32_bf16 v[196:199], a[0:3], a[120:123], v[196:199]\n\t"
    "ds_read_b128 a[136:139], v224 offset:55296\n\t"
    "v_exp_f32 v238, v238\n\t"
    "v_mfma_f32_16x16x32_bf16 v[196:199], a[4:7], a[124:127], v[196:199]\n\t"
    "ds_read_b128 a[140:143], v225 offset:55296\n\t"
    "v_exp_f32 v239, v239\n\t"
    "v_mfma_f32_16x16x32_bf16 v[200:203], a[8:11], a[120:123], v[200:203]\n\t"
    "v_exp_f32 v240, v240\n\t"
    "v_mfma_f32_16x16x32_bf16 v[200:203], a[12:15], a[124:127], v[200:203]\n\t"
    "v_exp_f32 v241, v241\n\t"
    "v_mfma_f32_16x16x32_bf16 v[204:207], a[16:19], a[120:123], v[204:207]\n\t"
    "v_exp_f32 v242, v242\n\t"
    "v_mfma_f32_16x16x32_bf16 v[204:207], a[20:23], a[124:127], v[204:207]\n\t"
    "v_exp_f32 v243, v243\n\t"
    "v_exp_f32 v244, v244\n\t"
    "v_exp_f32 v245, v245\n\t"
    "v_pk_add_f32 v[238:239], v[238:239], 1.0 op_sel_hi:[1,0]\n\t"
    "v_pk_add_f32 v[240:241], v[240:241], 1.0 op_sel_hi:[1,0]\n\t"
    "v_pk_add_f32 v[242:243], v[242:243], 1.0 op_sel_hi:[1,0]\n\t"
    "v_pk_add_f32 v[244:245], v[244:245], 1.0 op_sel_hi:[1,0]\n\t"
    "v_mfma_f32_16x16x32_bf16 v[208:211], a[24:27], a[120:123], v[208:211]\n\t"
    "v_rcp_f32 v238, v238\n\t"
    "v_mfma_f32_16x16x32_bf16 v[208:211], a[28:31], a[124:127], v[208:211]\n\t"
    "v_rcp_f32 v239, v239\n\t"
    "v_mfma_f32_16x16x32_bf16 v[212:215], a[32:35], a[120:123], v[212:215]\n\t"
    "v_rcp_f32 v240, v240\n\t"
    "v_mfma_f32_16x16x32_bf16 v[212:215], a[36:39], a[124:127], v[212:215]\n\t"
    "v_rcp_f32 v241, v241\n\t"
    "v_mfma_f32_16x16x32_bf16 v[216:219], a[40:43], a[120:123], v[216:219]\n\t"
    "v_rcp_f32 v242, v242\n\t"
    "v_mfma_f32_16x16x32_bf16 v[216:219], a[44:47], a[124:127], v[216:219]\n\t"
    "v_rcp_f32 v243, v243\n\t"
    "v_rcp_f32 v244, v244\n\t"
    "v_rcp_f32 v245, v245\n\t"
    "v_pk_mul_f32 v[116:117], v[116:117], v[128:129]\n\t"
    "v_pk_mul_f32 v[118:119], v[118:119], v[130:131]\n\t"
    "v_pk_mul_f32 v[144:145], v[144:145], v[156:157]\n\t"
    "v_pk_mul_f32 v[146:147], v[146:147], v[158:159]\n\t"
    "v_pk_mul_f32 v[116:117], v[116:117], v[238:239]\n\t"
    "v_pk_mul_f32 v[118:119], v[118:119], v[240:241]\n\t"
    "v_pk_mul_f32 v[144:145], v[144:145], v[242:243]\n\t"
    "s_waitcnt vmcnt(1)\n\t"
    "s_barrier\n\t"
    "s_cmp_lg_u32 s68, 0\n\t"
    "s_cbranch_scc1 F1P%=\n\t"
    "v_add_u32 v235, 1, v235\n\t"
    "s_mov_b32 exec_lo, 0\n\t"
    "s_mov_b32 exec_hi, 4\n\t"
    "buffer_store_dword v235, v248, s[52:55], 0 offen sc1\n\t"
    "s_mov_b64 exec, -1\n\t"
    "F1P%=:\n\t"
    "v_pk_mul_f32 v[146:147], v[146:147], v[244:245]\n\t"
    "v_cvt_pk_bf16_f32 v128, v116, v117\n\t"
    "v_cvt_pk_bf16_f32 v130, v144, v145\n\t"
    "v_cvt_pk_bf16_f32 v129, v118, v119\n\t"
    "v_cvt_pk_bf16_f32 v131, v146, v147\n\t"
    "v_pk_mul_f32 v[238:239], v[120:121], s[78:79] op_sel_hi:[1,0]\n\t"
    "v_pk_mul_f32 v[240:241], v[122:123], s[78:79] op_sel_hi:[1,0]\n\t"
    "v_pk_mul_f32 v[242:243], v[148:149], s[78:79] op_sel_hi:[1,0]\n\t"
    "v_pk_mul_f32 v[244:245], v[150:151], s[78:79] op_sel_hi:[1,0]\n\t"
    "v_permlane16_swap_b32 v128, v130\n\t"
    "v_permlane16_swap_b32 v129, v131\n\t"
    "s_mov_b32 s67, 458752\n\t"
    "buffer_store_dwordx4 v[128:131], v236, s[52:55], s67 offen offset:512 sc1\n\t"
    "v_mfma_f32_16x16x32_bf16 v[220:223], a[48:51], a[120:123], v[220:223]\n\t"
    "v_exp_f32 v238, v238\n\t"
    "v_mfma_f32_16x16x32_bf16 v[220:223], a[52:55], a[124:127], v[220:223]\n\t"
    "v_exp_f32 v239, v239\n\t"
    "s_waitcnt lgkmcnt(0)\n\t"
    "s_barrier\n\t"
    "v_mfma_f32_16x16x32_bf16 v[168:171], a[56:59], a[128:131], v[168:171]\n\t"
    "v_xor_b32 v225, 32, v251\n\t"
    "v_mov_b32 v224, v251\n\t"
    "v_add_u32 v226, 65536, v224\n\t"
    "v_add_u32 v227, 65536, v225\n\t"
    "v_add_u32 v228, 131072, v224\n\t"
    "v_add_u32 v229, 131072, v225\n\t"
    "v_exp_f32 v240, v240\n\t"
    "v_mfma_f32_16x16x32_bf16 v[168:171], a[60:63], a[132:135], v[168:171]\n\t"
    "s_mov_b32 s66, 1048576\n\t"
    "v_exp_f32 v241, v241\n\t"
    "v_mfma_f32_16x16x32_bf16 v[172:175], a[64:67], a[128:131], v[172:175]\n\t"
    "s_add_u32 m0, s64, 81920\n\t"
    "v_exp_f32 v242, v242\n\t"
    "v_mfma_f32_16x16x32_bf16 v[172:175], a[68:71], a[132:135], v[172:175]\n\t"
    "buffer_load_dwordx4 v246, s[48:51], s66 offen lds\n\t"
    "v_exp_f32 v243, v243\n\t"
    "v_exp_f32 v244, v244\n\t"
    "v_exp_f32 v245, v245\n\t"
    "v_pk_add_f32 v[238:239], v[238:239], 1.0 op_sel_hi:[1,0]\n\t"
    "v_pk_add_f32 v[240:241], v[240:241], 1.0 op_sel_hi:[1,0]\n\t"
    "v_pk_add_f32 v[242:243], v[242:243], 1.0 op_sel_hi:[1,0]\n\t"
    "v_pk_add_f32 v[244:245], v[244:245], 1.0 op_sel_hi:[1,0]\n\t"
    "v_mfma_f32_16x16x32_bf16 v[176:179], a[72:75], a[128:131], v[176:179]\n\t"
    "s_mov_b32 s66, 1114112\n\t"
    "v_rcp_f32 v238, v238\n\t"
    "v_mfma_f32_16x16x32_bf16 v[176:179], a[76:79], a[132:135], v[176:179]\n\t"
    "s_add_u32 m0, s64, 86016\n\t"
    "v_rcp_f32 v239, v239\n\t"
    "v_mfma_f32_16x16x32_bf16 v[180:183], a[80:83], a[128:131], v[180:183]\n\t"
    "buffer_load_dwordx4 v246, s[48:51], s66 offen lds\n\t"
    "v_rcp_f32 v240, v240\n\t"
    "v_mfma_f32_16x16x32_bf16 v[180:183], a[84:87], a[132:135], v[180:183]\n\t"
    "s_mov_b32 s66, 1179648\n\t"
    "v_rcp_f32 v241, v241\n\t"
    "v_mfma_f32_16x16x32_bf16 v[184:187], a[88:91], a[128:131], v[184:187]\n\t"
    "s_add_u32 m0, s64, 90112\n\t"
    "v_rcp_f32 v242, v242\n\t"
    "v_mfma_f32_16x16x32_bf16 v[184:187], a[92:95], a[132:135], v[184:187]\n\t"
    "buffer_load_dwordx4 v232, s[48:51], s66 offen lds\n\t"
    "v_rcp_f32 v243, v243\n\t"
    "v_rcp_f32 v244, v244\n\t"
    "v_rcp_f32 v245, v245\n\t"
    "v_pk_mul_f32 v[120:121], v[120:121], v[132:133]\n\t"
    "v_pk_mul_f32 v[122:123], v[122:123], v[134:135]\n\t"
    "v_pk_mul_f32 v[148:149], v[148:149], v[160:161]\n\t"
    "v_pk_mul_f32 v[150:151], v[150:151], v[162:163]\n\t"
    "v_pk_mul_f32 v[120:121], v[120:121], v[238:239]\n\t"
    "v_pk_mul_f32 v[122:123], v[122:123], v[240:241]\n\t"
    "v_pk_mul_f32 v[148:149], v[148:149], v[242:243]\n\t"
    "v_pk_mul_f32 v[150:151], v[150:151], v[244:245]\n\t"
    "v_cvt_pk_bf16_f32 v132, v120, v121\n\t"
    "v_cvt_pk_bf16_f32 v134, v148, v149\n\t"
    "v_cvt_pk_bf16_f32 v133, v122, v123\n\t"
    "v_cvt_pk_bf16_f32 v135, v150, v151\n\t"
    "s_nop 1\n\t"
    "v_permlane16_swap_b32 v132, v134\n\t"
    "v_permlane16_swap_b32 v133, v135\n\t"
    "s_mov_b32 s67, 458752\n\t"
    "buffer_store_dwordx4 v[132:135], v236, s[52:55], s67 offen offset:1024 sc1\n\t"
    "v_mfma_f32_16x16x32_bf16 v[188:191], a[96:99], a[128:131], v[188:191]\n\t"
    "s_mov_b32 s66, 1245184\n\t"
    "v_mfma_f32_16x16x32_bf16 v[188:191], a[100:103], a[132:135], v[188:191]\n\t"
    "s_add_u32 m0, s64, 94208\n\t"
    "v_mfma_f32_16x16x32_bf16 v[192:195], a[104:107], a[128:131], v[192:195]\n\t"
    "buffer_load_dwordx4 v232, s[48:51], s66 offen lds\n\t"
    "v_mfma_f32_16x16x32_bf16 v[192:195], a[108:111], a[132:135], v[192:195]\n\t"
    "s_mov_b32 s66, 1048576\n\t"
    "v_mfma_f32_16x16x32_bf16 v[196:199], a[56:59], a[136:139], v[196:199]\n\t"
    "s_add_u32 m0, s64, 114688\n\t"
    "v_mfma_f32_16x16x32_bf16 v[196:199], a[60:63], a[140:143], v[196:199]\n\t"
    "buffer_load_dwordx4 v247, s[48:51], s66 offen lds\n\t"
    "v_mfma_f32_16x16x32_bf16 v[200:203], a[64:67], a[136:139], v[200:203]\n\t"
    "s_mov_b32 s66, 1114112\n\t"
    "v_mfma_f32_16x16x32_bf16 v[200:203], a[68:71], a[140:143], v[200:203]\n\t"
    "s_add_u32 m0, s64, 118784\n\t"
    "v_mfma_f32_16x16x32_bf16 v[204:207], a[72:75], a[136:139], v[204:207]\n\t"
    "buffer_load_dwordx4 v247, s[48:51], s66 offen lds\n\t"
    "v_mfma_f32_16x16x32_bf16 v[204:207], a[76:79], a[140:143], v[204:207]\n\t"
    "s_mov_b32 s66, 1179648\n\t"
    "v_mfma_f32_16x16x32_bf16 v[208:211], a[80:83], a[136:139], v[208:211]\n\t"
    "s_add_u32 m0, s64, 122880\n\t"
    "v_mfma_f32_16x16x32_bf16 v[208:211], a[84:87], a[140:143], v[208:211]\n\t"
    "buffer_load_dwordx4 v233, s[48:51], s66 offen lds\n\t"
    "v_mfma_f32_16x16x32_bf16 v[212:215], a[88:91], a[136:139], v[212:215]\n\t"
    "s_mov_b32 s66, 1245184\n\t"
    "v_mfma_f32_16x16x32_bf16 v[212:215], a[92:95], a[140:143], v[212:215]\n\t"
    "s_add_u32 m0, s64, 126976\n\t"
    "v_mfma_f32_16x16x32_bf16 v[216:219], a[96:99], a[136:139], v[216:219]\n\t"
    "buffer_load_dwordx4 v233, s[48:51], s66 offen lds\n\t"
    "v_mfma_f32_16x16x32_bf16 v[216:219], a[100:103], a[140:143], v[216:219]\n\t"
    "v_mfma_f32_16x16x32_bf16 v[220:223], a[104:107], a[136:139], v[220:223]\n\t"
    "v_mfma_f32_16x16x32_bf16 v[220:223], a[108:111], a[140:143], v[220:223]\n\t"
    "s_nop 7\n\t"
    "v_pk_mul_f32 v[238:239], v[168:169], s[78:79] op_sel_hi:[1,0]\n\t"
    "v_pk_mul_f32 v[240:241], v[170:171], s[78:79] op_sel_hi:[1,0]\n\t"
    "v_pk_mul_f32 v[242:243], v[196:197], s[78:79] op_sel_hi:[1,0]\n\t"
    "v_pk_mul_f32 v[244:245], v[198:199], s[78:79] op_sel_hi:[1,0]\n\t"
    "v_exp_f32 v238, v238\n\t"
    "v_exp_f32 v239, v239\n\t"
    "v_exp_f32 v240, v240\n\t"
    "ST3_%=:\n\t"
    "s_cbranch_execz ST7_%=\n\t"
    "v_exp_f32 v241, v241\n\t"
    "v_exp_f32 v242, v242\n\t"
    "v_exp_f32 v243, v243\n\t"
    "v_exp_f32 v244, v244\n\t"
    "v_exp_f32 v245, v245\n\t"
    "v_pk_add_f32 v[238:239], v[238:239], 1.0 op_sel_hi:[1,0]\n\t"
    "v_pk_add_f32 v[240:241], v[240:241], 1.0 op_sel_hi:[1,0]\n\t"
    "v_pk_add_f32 v[242:243], v[242:243], 1.0 op_sel_hi:[1,0]\n\t"
    "v_pk_add_f32 v[244:245], v[244:245], 1.0 op_sel_hi:[1,0]\n\t"
    "v_rcp_f32 v238, v238\n\t"
    "v_rcp_f32 v239, v239\n\t"
    "v_rcp_f32 v240, v240\n\t"
    "v_rcp_f32 v241, v241\n\t"
    "v_rcp_f32 v242, v242\n\t"
    "v_rcp_f32 v243, v243\n\t"
    "v_rcp_f32 v244, v244\n\t"
    "v_rcp_f32 v245, v245\n\t"
    "v_pk_mul_f32 v[168:169], v[168:169], v[180:181]\n\t"
    "v_pk_mul_f32 v[170:171], v[170:171], v[182:183]\n\t"
    "v_pk_mul_f32 v[196:197], v[196:197], v[208:209]\n\t"
    "v_pk_mul_f32 v[198:199], v[198:199], v[210:211]\n\t"
    "v_pk_mul_f32 v[168:169], v[168:169], v[238:239]\n\t"
    "v_pk_mul_f32 v[170:171], v[170:171], v[240:241]\n\t"
    "v_pk_mul_f32 v[196:197], v[196:197], v[242:243]\n\t"
    "v_pk_mul_f32 v[198:199], v[198:199], v[244:245]\n\t"
    "v_cvt_pk_bf16_f32 v180, v168, v169\n\t"
    "v_cvt_pk_bf16_f32 v182, v196, v197\n\t"
    "v_cvt_pk_bf16_f32 v181, v170, v171\n\t"
    "v_cvt_pk_bf16_f32 v183, v198, v199\n\t"
    "v_pk_mul_f32 v[238:239], v[172:173], s[78:79] op_sel_hi:[1,0]\n\t"
    "v_pk_mul_f32 v[240:241], v[174:175], s[78:79] op_sel_hi:[1,0]\n\t"
    "v_pk_mul_f32 v[242:243], v[200:201], s[78:79] op_sel_hi:[1,0]\n\t"
    "v_pk_mul_f32 v[244:245], v[202:203], s[78:79] op_sel_hi:[1,0]\n\t"
    "v_permlane16_swap_b32 v180, v182\n\t"
    "v_permlane16_swap_b32 v181, v183\n\t"
    "s_mov_b32 s67, 688128\n\t"
    "buffer_store_dwordx4 v[180:183], v236, s[52:55], s67 offen offset:0 sc1\n\t"
    "v_exp_f32 v238, v238\n\t"
    "v_exp_f32 v239, v239\n\t"
    "v_exp_f32 v240, v240\n\t"
    "v_exp_f32 v241, v241\n\t"
    "v_exp_f32 v242, v242\n\t"
    "v_exp_f32 v243, v243\n\t"
    "v_exp_f32 v244, v244\n\t"
    "v_exp_f32 v245, v245\n\t"
    "v_pk_add_f32 v[238:239], v[238:239], 1.0 op_sel_hi:[1,0]\n\t"
    "v_pk_add_f32 v[240:241], v[240:241], 1.0 op_sel_hi:[1,0]\n\t"
    "v_pk_add_f32 v[242:243], v[242:243], 1.0 op_sel_hi:[1,0]\n\t"
    "v_pk_add_f32 v[244:245], v[244:245], 1.0 op_sel_hi:[1,0]\n\t"
    "v_rcp_f32 v238, v238\n\t"
    "v_rcp_f32 v239, v239\n\t"
    "v_rcp_f32 v240, v240\n\t"
    "v_rcp_f32 v241, v241\n\t"
    "v_rcp_f32 v242, v242\n\t"
    "v_rcp_f32 v243, v243\n\t"
    "v_rcp_f32 v244, v244\n\t"
    "v_rcp_f32 v245, v245\n\t"
    "v_pk_mul_f32 v[172:173], v[172:173], v[184:185]\n\t"
    "v_pk_mul_f32 v[174:175], v[174:175], v[186:187]\n\t"
    "v_pk_mul_f32 v[200:201], v[200:201], v[212:213]\n\t"
    "v_pk_mul_f32 v[202:203], v[202:203], v[214:215]\n\t"
    "v_pk_mul_f32 v[172:173], v[172:173], v[238:239]\n\t"
    "v_pk_mul_f32 v[174:175], v[174:175], v[240:241]\n\t"
    "v_pk_mul_f32 v[200:201], v[200:201], v[242:243]\n\t"
    "v_pk_mul_f32 v[202:203], v[202:203], v[244:245]\n\t"
    "v_cvt_pk_bf16_f32 v184, v172, v173\n\t"
    "v_cvt_pk_bf16_f32 v186, v200, v201\n\t"
    "v_cvt_pk_bf16_f32 v185, v174, v175\n\t"
    "v_cvt_pk_bf16_f32 v187, v202, v203\n\t"
    "v_pk_mul_f32 v[238:239], v[176:177], s[78:79] op_sel_hi:[1,0]\n\t"
    "v_pk_mul_f32 v[240:241], v[178:179], s[78:79] op_sel_hi:[1,0]\n\t"
    "v_pk_mul_f32 v[242:243], v[204:205], s[78:79] op_sel_hi:[1,0]\n\t"
    "v_pk_mul_f32 v[244:245], v[206:207], s[78:79] op_sel_hi:[1,0]\n\t"
    "v_permlane16_swap_b32 v184, v186\n\t"
    "v_permlane16_swap_b32 v185, v187\n\t"
    "s_mov_b32 s67, 688128\n\t"
    "buffer_store_dwordx4 v[184:187], v236, s[52:55], s67 offen offset:512 sc1\n\t"
    "v_exp_f32 v238, v238\n\t"
    "v_exp_f32 v239, v239\n\t"
    "v_exp_f32 v240, v240\n\t"
    "v_exp_f32 v241, v241\n\t"
    "v_exp_f32 v242, v242\n\t"
    "v_exp_f32 v243, v243\n\t"
    "v_exp_f32 v244, v244\n\t"
    "v_exp_f32 v245, v245\n\t"
    "v_pk_add_f32 v[238:239], v[238:239], 1.0 op_sel_hi:[1,0]\n\t"
    "v_pk_add_f32 v[240:241], v[240:241], 1.0 op_sel_hi:[1,0]\n\t"
    "v_pk_add_f32 v[242:243], v[242:243], 1.0 op_sel_hi:[1,0]\n\t"
    "v_pk_add_f32 v[244:245], v[244:245], 1.0 op_sel_hi:[1,0]\n\t"
    "s_mov_b64 exec, 65535\n\t"
    "buffer_load_dword v250, v248, s[52:55], 0 offen sc1\n\t"
    "s_mov_b64 exec, -1\n\t"
    "v_rcp_f32 v238, v238\n\t"
    "v_rcp_f32 v239, v239\n\t"
    "v_rcp_f32 v240, v240\n\t"
    "v_rcp_f32 v241, v241\n\t"
    "v_rcp_f32 v242, v242\n\t"
    "v_rcp_f32 v243, v243\n\t"
    "v_rcp_f32 v244, v244\n\t"
    "v_rcp_f32 v245, v245\n\t"
    "v_pk_mul_f32 v[176:177], v[176:177], v[188:189]\n\t"
    "v_pk_mul_f32 v[178:179], v[178:179], v[190:191]\n\t"
    "v_pk_mul_f32 v[204:205], v[204:205], v[216:217]\n\t"
    "v_pk_mul_f32 v[206:207], v[206:207], v[218:219]\n\t"
    "v_pk_mul_f32 v[176:177], v[176:177], v[238:239]\n\t"
    "v_pk_mul_f32 v[178:179], v[178:179], v[240:241]\n\t"
    "v_pk_mul_f32 v[204:205], v[204:205], v[242:243]\n\t"
    "v_pk_mul_f32 v[206:207], v[206:207], v[244:245]\n\t"
    "v_cvt_pk_bf16_f32 v188, v176, v177\n\t"
    "v_cvt_pk_bf16_f32 v190, v204, v205\n\t"
    "v_cvt_pk_bf16_f32 v189, v178, v179\n\t"
    "v_cvt_pk_bf16_f32 v191, v206, v207\n\t"
    "v_permlane32_swap_b32 v136, v164\n\t"
    "v_permlane32_swap_b32 v137, v165\n\t"
    "v_permlane32_swap_b32 v138, v166\n\t"
    "v_permlane32_swap_b32 v139, v167\n\t"
    "v_permlane32_swap_b32 v192, v220\n\t"
    "v_permlane32_swap_b32 v193, v221\n\t"
    "v_permlane32_swap_b32 v194, v222\n\t"
    "v_permlane32_swap_b32 v195, v223\n\t"
    "v_pk_mul_f32 v[238:239], v[136:137], s[78:79] op_sel_hi:[1,0]\n\t"
    "v_pk_mul_f32 v[240:241], v[138:139], s[78:79] op_sel_hi:[1,0]\n\t"
    "v_pk_mul_f32 v[242:243], v[192:193], s[78:79] op_sel_hi:[1,0]\n\t"
    "v_pk_mul_f32 v[244:245], v[194:195], s[78:79] op_sel_hi:[1,0]\n\t"
    "v_permlane16_swap_b32 v188, v190\n\t"
    "v_permlane16_swap_b32 v189, v191\n\t"
    "s_mov_b32 s67, 688128\n\t"
    "buffer_store_dwordx4 v[188:191], v236, s[52:55], s67 offen offset:1024 sc1\n\t"
    "v_exp_f32 v238, v238\n\t"
    "v_exp_f32 v239, v239\n\t"
    "v_exp_f32 v240, v240\n\t"
    "v_exp_f32 v241, v241\n\t"
    "v_exp_f32 v242, v242\n\t"
    "v_exp_f32 v243, v243\n\t"
    "v_exp_f32 v244, v244\n\t"
    "v_exp_f32 v245, v245\n\t"
    "v_pk_add_f32 v[238:239], v[238:239], 1.0 op_sel_hi:[1,0]\n\t"
    "v_pk_add_f32 v[240:241], v[240:241], 1.0 op_sel_hi:[1,0]\n\t"
    "v_pk_add_f32 v[242:243], v[242:243], 1.0 op_sel_hi:[1,0]\n\t"
    "v_pk_add_f32 v[244:245], v[244:245], 1.0 op_sel_hi:[1,0]\n\t"
    "v_rcp_f32 v238, v238\n\t"
    "v_rcp_f32 v239, v239\n\t"
    "v_rcp_f32 v240, v240\n\t"
    "v_rcp_f32 v241, v241\n\t"
    "v_rcp_f32 v242, v242\n\t"
    "v_rcp_f32 v243, v243\n\t"
    "v_rcp_f32 v244, v244\n\t"
    "v_rcp_f32 v245, v245\n\t"
    "v_pk_mul_f32 v[136:137], v[136:137], v[164:165]\n\t"
    "v_pk_mul_f32 v[138:139], v[138:139], v[166:167]\n\t"
    "v_pk_mul_f32 v[192:193], v[192:193], v[220:221]\n\t"
    "v_pk_mul_f32 v[194:195], v[194:195], v[222:223]\n\t"
    "v_pk_mul_f32 v[136:137], v[136:137], v[238:239]\n\t"
    "v_pk_mul_f32 v[138:139], v[138:139], v[240:241]\n\t"
    "v_pk_mul_f32 v[192:193], v[192:193], v[242:243]\n\t"
    "v_pk_mul_f32 v[194:195], v[194:195], v[244:245]\n\t"
    "v_cvt_pk_bf16_f32 v164, v136, v137\n\t"
    "v_cvt_pk_bf16_f32 v166, v192, v193\n\t"
    "v_cvt_pk_bf16_f32 v165, v138, v139\n\t"
    "v_cvt_pk_bf16_f32 v167, v194, v195\n\t"
    "s_nop 1\n\t"
    "v_permlane16_swap_b32 v164, v166\n\t"
    "v_permlane16_swap_b32 v165, v167\n\t"
    "s_mov_b32 s67, 458752\n\t"
    "buffer_store_dwordx4 v[164:167], v237, s[52:55], s67 offen sc1\n\t"
    "ds_read_b64_tr_b16 v[128:129], v228 offset:0\n\t"
    "ds_read_b64_tr_b16 v[130:131], v228 offset:512\n\t"
    "ds_read_b64_tr_b16 v[132:133], v228 offset:4096\n\t"
    "ds_read_b64_tr_b16 v[134:135], v228 offset:4608\n\t"
    "ds_read_b64_tr_b16 v[136:137], v229 offset:0\n\t"
    "ds_read_b64_tr_b16 v[138:139], v229 offset:512\n\t"
    "ds_read_b64_tr_b16 v[140:141], v229 offset:4096\n\t"
    "ds_read_b64_tr_b16 v[142:143], v229 offset:4608\n\t"
    "s_waitcnt vmcnt(2)\n\t"
    "v_cmp_ne_u32 vcc, v250, v249\n\t"
    "s_nop 0\n\t"
    "s_and_b32 s71, vcc_lo, 65535\n\t"
    "s_cmp_eq_u32 s71, 65535\n\t"
    "s_cbranch_scc0 HS%=\n\t"
    "s_mov_b32 s67, 0\n\t"
    "buffer_load_dwordx4 a[240:243], v252, s[52:55], s67 offen offset:0 sc1\n\t"
    "buffer_load_dwordx4 a[244:247], v252, s[52:55], s67 offen offset:1024 sc1\n\t"
    "buffer_load_dwordx4 a[248:251], v230, s[52:55], s67 offen offset:0 sc1\n\t"
    "buffer_load_dwordx4 a[252:255], v230, s[52:55], s67 offen offset:1024 sc1\n\t"
    "s_mov_b32 s67, 4096\n\t"
    "buffer_load_dwordx4 a[224:227], v252, s[52:55], s67 offen offset:0 sc1\n\t"
    "buffer_load_dwordx4 a[228:231], v252, s[52:55], s67 offen offset:1024 sc1\n\t"
    "buffer_load_dwordx4 a[232:235], v230, s[52:55], s67 offen offset:0 sc1\n\t"
    "buffer_load_dwordx4 a[236:239], v230, s[52:55], s67 offen offset:1024 sc1\n\t"
    "s_mov_b32 s67, 8192\n\t"
    "buffer_load_dwordx4 a[208:211], v252, s[52:55], s67 offen offset:0 sc1\n\t"
    "buffer_load_dwordx4 a[212:215], v252, s[52:55], s67 offen offset:1024 sc1\n\t"
    "buffer_load_dwordx4 a[216:219], v230, s[52:55], s67 offen offset:0 sc1\n\t"
    "buffer_load_dwordx4 a[220:223], v230, s[52:55], s67 offen offset:1024 sc1\n\t"
    "s_mov_b32 s67, 12288\n\t"
    "buffer_load_dwordx4 a[192:195], v252, s[52:55], s67 offen offset:0 sc1\n\t"
    "buffer_load_dwordx4 a[196:199], v252, s[52:55], s67 offen offset:1024 sc1\n\t"
    "buffer_load_dwordx4 a[200:203], v230, s[52:55], s67 offen offset:0 sc1\n\t"
    "buffer_load_dwordx4 a[204:207], v230, s[52:55], s67 offen offset:1024 sc1\n\t"
    "s_waitcnt vmcnt(16)\n\t"
    "s_barrier\n\t"
    "s_cmp_lg_u32 s68, 0\n\t"
    "s_cbranch_scc1 F2P%=\n\t"
    "v_add_u32 v235, 1, v235\n\t"
    "s_mov_b32 exec_lo, 0\n\t"
    "s_mov_b32 exec_hi, 8\n\t"
    "buffer_store_dword v235, v248, s[52:55], 0 offen sc1\n\t"
    "s_mov_b64 exec, -1\n\t"
    "F2P%=:\n\t"
    "HJ%=:\n\t"
    "s_waitcnt vmcnt(12) lgkmcnt(4)\n\t"
    "v_mfma_f32_16x16x32_bf16 v[0:3], v[128:131], a[240:243], 0\n\t"
    "ds_read_b64_tr_b16 v[144:145], v228 offset:256\n\t"
    "v_mfma_f32_16x16x32_bf16 v[0:3], v[132:135], a[244:247], v[0:3]\n\t"
    "ds_read_b64_tr_b16 v[146:147], v228 offset:768\n\t"
    "s_mov_b32 s67, 16384\n\t"
    "v_mfma_f32_16x16x32_bf16 v[4:7], v[128:131], a[248:251], 0\n\t"
    "ds_read_b64_tr_b16 v[148:149], v228 offset:4352\n\t"
    "buffer_load_dwordx4 a[32:35], v252, s[52:55], s67 offen offset:0 sc1\n\t"
    "v_mfma_f32_16x16x32_bf16 v[4:7], v[132:135], a[252:255], v[4:7]\n\t"
    "ds_read_b64_tr_b16 v[150:151], v228 offset:4864\n\t"
    "s_waitcnt lgkmcnt(4)\n\t"
    "v_mfma_f32_16x16x32_bf16 v[16:19], v[136:139], a[240:243], 0\n\t"
    "ds_read_b64_tr_b16 v[152:153], v229 offset:256\n\t"
    "v_mfma_f32_16x16x32_bf16 v[16:19], v[140:143], a[244:247], v[16:19]\n\t"
    "ds_read_b64_tr_b16 v[154:155], v229 offset:768\n\t"
    "v_mfma_f32_16x16x32_bf16 v[20:23], v[136:139], a[248:251], 0\n\t"
    "ds_read_b64_tr_b16 v[156:157], v229 offset:4352\n\t"
    "buffer_load_dwordx4 a[36:39], v252, s[52:55], s67 offen offset:1024 sc1\n\t"
    "v_mfma_f32_16x16x32_bf16 v[20:23], v[140:143], a[252:255], v[20:23]\n\t"
    "ds_read_b64_tr_b16 v[158:159], v229 offset:4864\n\t"
    "s_waitcnt lgkmcnt(4)\n\t"
    "v_mfma_f32_16x16x32_bf16 v[32:35], v[144:147], a[240:243], 0\n\t"
    "ds_read_b64_tr_b16 v[128:129], v228 offset:16384\n\t"
    "v_mfma_f32_16x16x32_bf16 v[32:35], v[148:151], a[244:247], v[32:35]\n\t"
    "ds_read_b64_tr_b16 v[130:131], v228 offset:16896\n\t"
    "v_mfma_f32_16x16x32_bf16 v[36:39], v[144:147], a[248:251], 0\n\t"
    "ds_read_b64_tr_b16 v[132:133], v228 offset:20480\n\t"
    "buffer_load_dwordx4 a[40:43], v230, s[52:55], s67 offen offset:0 sc1\n\t"
    "v_mfma_f32_16x16x32_bf16 v[36:39], v[148:151], a[252:255], v[36:39]\n\t"
    "ds_read_b64_tr_b16 v[134:135], v228 offset:20992\n\t"
    "s_waitcnt lgkmcnt(4)\n\t"
    "v_mfma_f32_16x16x32_bf16 v[48:51], v[152:155], a[240:243], 0\n\t"
    "ds_read_b64_tr_b16 v[136:137], v229 offset:16384\n\t"
    "v_mfma_f32_16x16x32_bf16 v[48:51], v[156:159], a[244:247], v[48:51]\n\t"
    "ds_read_b64_tr_b16 v[138:139], v229 offset:16896\n\t"
    "v_mfma_f32_16x16x32_bf16 v[52:55], v[152:155], a[248:251], 0\n\t"
    "ds_read_b64_tr_b16 v[140:141], v229 offset:20480\n\t"
    "buffer_load_dwordx4 a[44:47], v230, s[52:55], s67 offen offset:1024 sc1\n\t"
    "v_mfma_f32_16x16x32_bf16 v[52:55], v[156:159], a[252:255], v[52:55]\n\t"
    "ds_read_b64_tr_b16 v[142:143], v229 offset:20992\n\t"
    "s_waitcnt lgkmcnt(4)\n\t"
    "v_mfma_f32_16x16x32_bf16 v[64:67], v[128:131], a[240:243], 0\n\t"
    "ds_read_b64_tr_b16 v[144:145], v228 offset:16640\n\t"
    "v_mfma_f32_16x16x32_bf16 v[64:67], v[132:135], a[244:247], v[64:67]\n\t"
    "ds_read_b64_tr_b16 v[146:147], v228 offset:17152\n\t"
    "s_mov_b32 s67, 20480\n\t"
    "v_mfma_f32_16x16x32_bf16 v[68:71], v[128:131], a[248:251], 0\n\t"
    "ds_read_b64_tr_b16 v[148:149], v228 offset:20736\n\t"
    "buffer_load_dwordx4 a[176:179], v252, s[52:55], s67 offen offset:0 sc1\n\t"
    "v_mfma_f32_16x16x32_bf16 v[68:71], v[132:135], a[252:255], v[68:71]\n\t"
    "ds_read_b64_tr_b16 v[150:151], v228 offset:21248\n\t"
    "s_waitcnt lgkmcnt(4)\n\t"
    "v_mfma_f32_16x16x32_bf16 v[80:83], v[136:139], a[240:243], 0\n\t"
    "ds_read_b64_tr_b16 v[152:153], v229 offset:16640\n\t"
    "v_mfma_f32_16x16x32_bf16 v[80:83], v[140:143], a[244:247], v[80:83]\n\t"
    "ds_read_b64_tr_b16 v[154:155], v229 offset:17152\n\t"
    "v_mfma_f32_16x16x32_bf16 v[84:87], v[136:139], a[248:251], 0\n\t"
    "ds_read_b64_tr_b16 v[156:157], v229 offset:20736\n\t"
    "buffer_load_dwordx4 a[180:183], v252, s[52:55], s67 offen offset:1024 sc1\n\t"
    "v_mfma_f32_16x16x32_bf16 v[84:87], v[140:143], a[252:255], v[84:87]\n\t"
    "ds_read_b64_tr_b16 v[158:159], v229 offset:21248\n\t"
    "s_waitcnt lgkmcnt(4)\n\t"
    "v_mfma_f32_16x16x32_bf16 v[96:99], v[144:147], a[240:243], 0\n\t"
    "ds_read_b64_tr_b16 v[128:129], v224 offset:0\n\t"
    "v_mfma_f32_16x16x32_bf16 v[96:99], v[148:151], a[244:247], v[96:99]\n\t"
    "ds_read_b64_tr_b16 v[130:131], v224 offset:512\n\t"
    "v_mfma_f32_16x16x32_bf16 v[100:103], v[144:147], a[248:251], 0\n\t"
    "ds_read_b64_tr_b16 v[132:133], v224 offset:4096\n\t"
    "buffer_load_dwordx4 a[184:187], v230, s[52:55], s67 offen offset:0 sc1\n\t"
    "v_mfma_f32_16x16x32_bf16 v[100:103], v[148:151], a[252:255], v[100:103]\n\t"
    "ds_read_b64_tr_b16 v[134:135], v224 offset:4608\n\t"
    "s_waitcnt lgkmcnt(4)\n\t"
    "v_mfma_f32_16x16x32_bf16 v[112:115], v[152:155], a[240:243], 0\n\t"
    "ds_read_b64_tr_b16 v[136:137], v225 offset:0\n\t"
    "v_mfma_f32_16x16x32_bf16 v[112:115], v[156:159], a[244:247], v[112:115]\n\t"
    "ds_read_b64_tr_b16 v[138:139], v225 offset:512\n\t"
    "v_mfma_f32_16x16x32_bf16 v[116:119], v[152:155], a[248:251], 0\n\t"
    "ds_read_b64_tr_b16 v[140:141], v225 offset:4096\n\t"
    "buffer_load_dwordx4 a[188:191], v230, s[52:55], s67 offen offset:1024 sc1\n\t"
    "v_mfma_f32_16x16x32_bf16 v[116:119], v[156:159], a[252:255], v[116:119]\n\t"
    "ds_read_b64_tr_b16 v[142:143], v225 offset:4608\n\t"
    "s_waitcnt vmcnt(16) lgkmcnt(4)\n\t"
    "v_mfma_f32_16x16x32_bf16 v[0:3], v[128:131], a[224:227], v[0:3]\n\t"
    "ds_read_b64_tr_b16 v[144:145], v224 offset:256\n\t"
    "v_mfma_f32_16x16x32_bf16 v[0:3], v[132:135], a[228:231], v[0:3]\n\t"
    "ds_read_b64_tr_b16 v[146:147], v224 offset:768\n\t"
    "s_mov_b32 s67, 24576\n\t"
    "v_mfma_f32_16x16x32_bf16 v[4:7], v[128:131], a[232:235], v[4:7]\n\t"
    "ds_read_b64_tr_b16 v[148:149], v224 offset:4352\n\t"
    "buffer_load_dwordx4 a[160:163], v252, s[52:55], s67 offen offset:0 sc1\n\t"
    "v_mfma_f32_16x16x32_bf16 v[4:7], v[132:135], a[236:239], v[4:7]\n\t"
    "ds_read_b64_tr_b16 v[150:151], v224 offset:4864\n\t"
    "s_waitcnt lgkmcnt(4)\n\t"
    "v_mfma_f32_16x16x32_bf16 v[16:19], v[136:139], a[224:227], v[16:19]\n\t"
    "ds_read_b64_tr_b16 v[152:153], v225 offset:256\n\t"
    "v_mfma_f32_16x16x32_bf16 v[16:19], v[140:143], a[228:231], v[16:19]\n\t"
    "ds_read_b64_tr_b16 v[154:155], v225 offset:768\n\t"
    "v_mfma_f32_16x16x32_bf16 v[20:23], v[136:139], a[232:235], v[20:23]\n\t"
    "ds_read_b64_tr_b16 v[156:157], v225 offset:4352\n\t"
    "buffer_load_dwordx4 a[164:167], v252, s[52:55], s67 offen offset:1024 sc1\n\t"
    "v_mfma_f32_16x16x32_bf16 v[20:23], v[140:143], a[236:239], v[20:23]\n\t"
    "ds_read_b64_tr_b16 v[158:159], v225 offset:4864\n\t"
    "s_waitcnt lgkmcnt(4)\n\t"
    "v_mfma_f32_16x16x32_bf16 v[32:35], v[144:147], a[224:227], v[32:35]\n\t"
    "ds_read_b64_tr_b16 v[128:129], v224 offset:16384\n\t"
    "v_mfma_f32_16x16x32_bf16 v[32:35], v[148:151], a[228:231], v[32:35]\n\t"
    "ds_read_b64_tr_b16 v[130:131], v224 offset:16896\n\t"
    "v_mfma_f32_16x16x32_bf16 v[36:39], v[144:147], a[232:235], v[36:39]\n\t"
    "ds_read_b64_tr_b16 v[132:133], v224 offset:20480\n\t"
    "buffer_load_dwordx4 a[168:171], v230, s[52:55], s67 offen offset:0 sc1\n\t"
    "v_mfma_f32_16x16x32_bf16 v[36:39], v[148:151], a[236:239], v[36:39]\n\t"
    "ds_read_b64_tr_b16 v[134:135], v224 offset:20992\n\t"
    "s_waitcnt lgkmcnt(4)\n\t"
    "v_mfma_f32_16x16x32_bf16 v[48:51], v[152:155], a[224:227], v[48:51]\n\t"
    "ds_read_b64_tr_b16 v[136:137], v225 offset:16384\n\t"
    "v_mfma_f32_16x16x32_bf16 v[48:51], v[156:159], a[228:231], v[48:51]\n\t"
    "ds_read_b64_tr_b16 v[138:139], v225 offset:16896\n\t"
    "v_mfma_f32_16x16x32_bf16 v[52:55], v[152:155], a[232:235], v[52:55]\n\t"
    "ds_read_b64_tr_b16 v[140:141], v225 offset:20480\n\t"
    "buffer_load_dwordx4 a[172:175], v230, s[52:55], s67 offen offset:1024 sc1\n\t"
    "v_mfma_f32_16x16x32_bf16 v[52:55], v[156:159], a[236:239], v[52:55]\n\t"
    "ds_read_b64_tr_b16 v[142:143], v225 offset:20992\n\t"
    "s_waitcnt lgkmcnt(4)\n\t"
    "v_mfma_f32_16x16x32_bf16 v[64:67], v[128:131], a[224:227], v[64:67]\n\t"
    "ds_read_b64_tr_b16 v[144:145], v224 offset:16640\n\t"
    "v_mfma_f32_16x16x32_bf16 v[64:67], v[132:135], a[228:231], v[64:67]\n\t"
    "ds_read_b64_tr_b16 v[146:147], v224 offset:17152\n\t"
    "s_mov_b32 s67, 28672\n\t"
    "v_mfma_f32_16x16x32_bf16 v[68:71], v[128:131], a[232:235], v[68:71]\n\t"
    "ds_read_b64_tr_b16 v[148:149], v224 offset:20736\n\t"
    "buffer_load_dwordx4 a[144:147], v252, s[52:55], s67 offen offset:0 sc1\n\t"
    "v_mfma_f32_16x16x32_bf16 v[68:71], v[132:135], a[236:239], v[68:71]\n\t"
    "ds_read_b64_tr_b16 v[150:151], v224 offset:21248\n\t"
    "s_cmp_lg_u32 s68, 0\n\t"
    "s_cbranch_scc1 X1%=\n\t"
    "s_mov_b32 exec_lo, 4294901760\n\t"
    "s_mov_b32 exec_hi, 1\n\t"
    "buffer_load_dword v250, v248, s[52:55], 0 offen sc1\n\t"
    "s_mov_b64 exec, -1\n\t"
    "X1%=:\n\t"
    "s_waitcnt lgkmcnt(4)\n\t"
    "v_mfma_f32_16x16x32_bf16 v[80:83], v[136:139], a[224:227], v[80:83]\n\t"
    "ds_read_b64_tr_b16 v[152:153], v225 offset:16640\n\t"
    "v_mfma_f32_16x16x32_bf16 v[80:83], v[140:143], a[228:231], v[80:83]\n\t"
    "ds_read_b64_tr_b16 v[154:155], v225 offset:17152\n\t"
    "v_mfma_f32_16x16x32_bf16 v[84:87], v[136:139], a[232:235], v[84:87]\n\t"
    "ds_read_b64_tr_b16 v[156:157], v225 offset:20736\n\t"
    "buffer_load_dwordx4 a[148:151], v252, s[52:55], s67 offen offset:1024 sc1\n\t"
    "v_mfma_f32_16x16x32_bf16 v[84:87], v[140:143], a[236:239], v[84:87]\n\t"
    "ds_read_b64_tr_b16 v[158:159], v225 offset:21248\n\t"
    "s_waitcnt lgkmcnt(4)\n\t"
    "v_mfma_f32_16x16x32_bf16 v[96:99], v[144:147], a[224:227], v[96:99]\n\t"
    "ds_read_b64_tr_b16 v[128:129], v226 offset:0\n\t"
    "v_mfma_f32_16x16x32_bf16 v[96:99], v[148:151], a[228:231], v[96:99]\n\t"
    "ds_read_b64_tr_b16 v[130:131], v226 offset:512\n\t"
    "v_mfma_f32_16x16x32_bf16 v[100:103], v[144:147], a[232:235], v[100:103]\n\t"
    "ds_read_b64_tr_b16 v[132:133], v226 offset:4096\n\t"
    "buffer_load_dwordx4 a[152:155], v230, s[52:55], s67 offen offset:0 sc1\n\t"
    "v_mfma_f32_16x16x32_bf16 v[100:103], v[148:151], a[236:239], v[100:103]\n\t"
    "ds_read_b64_tr_b16 v[134:135], v226 offset:4608\n\t"
    "s_waitcnt lgkmcnt(4)\n\t"
    "v_mfma_f32_16x16x32_bf16 v[112:115], v[152:155], a[224:227], v[112:115]\n\t"
    "ds_read_b64_tr_b16 v[136:137], v227 offset:0\n\t"
    "v_mfma_f32_16x16x32_bf16 v[112:115], v[156:159], a[228:231], v[112:115]\n\t"
    "ds_read_b64_tr_b16 v[138:139], v227 offset:512\n\t"
    "v_mfma_f32_16x16x32_bf16 v[116:119], v[152:155], a[232:235], v[116:119]\n\t"
    "ds_read_b64_tr_b16 v[140:141], v227 offset:4096\n\t"
    "buffer_load_dwordx4 a[156:159], v230, s[52:55], s67 offen offset:1024 sc1\n\t"
    "v_mfma_f32_16x16x32_bf16 v[116:119], v[156:159], a[236:239], v[116:119]\n\t"
    "ds_read_b64_tr_b16 v[142:143], v227 offset:4608\n\t"
    "s_waitcnt vmcnt(20) lgkmcnt(4)\n\t"
    "v_mfma_f32_16x16x32_bf16 v[0:3], v[128:131], a[208:211], v[0:3]\n\t"
    "ds_read_b64_tr_b16 v[144:145], v226 offset:256\n\t"
    "v_mfma_f32_16x16x32_bf16 v[0:3], v[132:135], a[212:215], v[0:3]\n\t"
    "ds_read_b64_tr_b16 v[146:147], v226 offset:768\n\t"
    "s_mov_b32 s67, 32768\n\t"
    "v_mfma_f32_16x16x32_bf16 v[4:7], v[128:131], a[216:219], v[4:7]\n\t"
    "ds_read_b64_tr_b16 v[148:149], v226 offset:4352\n\t"
    "buffer_load_dwordx4 a[128:131], v252, s[52:55], s67 offen offset:0 sc1\n\t"
    "v_mfma_f32_16x16x32_bf16 v[4:7], v[132:135], a[220:223], v[4:7]\n\t"
    "ds_read_b64_tr_b16 v[150:151], v226 offset:4864\n\t"
    "s_waitcnt lgkmcnt(4)\n\t"
    "v_mfma_f32_16x16x32_bf16 v[16:19], v[136:139], a[208:211], v[16:19]\n\t"
    "ds_read_b64_tr_b16 v[152:153], v227 offset:256\n\t"
    "v_mfma_f32_16x16x32_bf16 v[16:19], v[140:143], a[212:215], v[16:19]\n\t"
    "ds_read_b64_tr_b16 v[154:155], v227 offset:768\n\t"
    "v_mfma_f32_16x16x32_bf16 v[20:23], v[136:139], a[216:219], v[20:23]\n\t"
    "ds_read_b64_tr_b16 v[156:157], v227 offset:4352\n\t"
    "buffer_load_dwordx4 a[132:135], v252, s[52:55], s67 offen offset:1024 sc1\n\t"
    "v_mfma_f32_16x16x32_bf16 v[20:23], v[140:143], a[220:223], v[20:23]\n\t"
    "ds_read_b64_tr_b16 v[158:159], v227 offset:4864\n\t"
    "s_waitcnt lgkmcnt(4)\n\t"
    "v_mfma_f32_16x16x32_bf16 v[32:35], v[144:147], a[208:211], v[32:35]\n\t"
    "ds_read_b64_tr_b16 v[128:129], v224 offset:32768\n\t"
    "v_mfma_f32_16x16x32_bf16 v[32:35], v[148:151], a[212:215], v[32:35]\n\t"
    "ds_read_b64_tr_b16 v[130:131], v224 offset:33280\n\t"
    "v_mfma_f32_16x16x32_bf16 v[36:39], v[144:147], a[216:219], v[36:39]\n\t"
    "ds_read_b64_tr_b16 v[132:133], v224 offset:36864\n\t"
    "buffer_load_dwordx4 a[136:139], v230, s[52:55], s67 offen offset:0 sc1\n\t"
    "v_mfma_f32_16x16x32_bf16 v[36:39], v[148:151], a[220:223], v[36:39]\n\t"
    "ds_read_b64_tr_b16 v[134:135], v224 offset:37376\n\t"
    "s_waitcnt lgkmcnt(4)\n\t"
    "v_mfma_f32_16x16x32_bf16 v[48:51], v[152:155], a[208:211], v[48:51]\n\t"
    "ds_read_b64_tr_b16 v[136:137], v225 offset:32768\n\t"
    "v_mfma_f32_16x16x32_bf16 v[48:51], v[156:159], a[212:215], v[48:51]\n\t"
    "ds_read_b64_tr_b16 v[138:139], v225 offset:33280\n\t"
    "v_mfma_f32_16x16x32_bf16 v[52:55], v[152:155], a[216:219], v[52:55]\n\t"
    "ds_read_b64_tr_b16 v[140:141], v225 offset:36864\n\t"
    "buffer_load_dwordx4 a[140:143], v230, s[52:55], s67 offen offset:1024 sc1\n\t"
    "v_mfma_f32_16x16x32_bf16 v[52:55], v[156:159], a[220:223], v[52:55]\n\t"
    "ds_read_b64_tr_b16 v[142:143], v225 offset:37376\n\t"
    "s_waitcnt lgkmcnt(4)\n\t"
    "v_mfma_f32_16x16x32_bf16 v[64:67], v[128:131], a[208:211], v[64:67]\n\t"
    "ds_read_b64_tr_b16 v[144:145], v224 offset:33024\n\t"
    "v_mfma_f32_16x16x32_bf16 v[64:67], v[132:135], a[212:215], v[64:67]\n\t"
    "ds_read_b64_tr_b16 v[146:147], v224 offset:33536\n\t"
    "s_mov_b32 s67, 36864\n\t"
    "v_mfma_f32_16x16x32_bf16 v[68:71], v[128:131], a[216:219], v[68:71]\n\t"
    "ds_read_b64_tr_b16 v[148:149], v224 offset:37120\n\t"
    "buffer_load_dwordx4 a[112:115], v252, s[52:55], s67 offen offset:0 sc1\n\t"
    "v_mfma_f32_16x16x32_bf16 v[68:71], v[132:135], a[220:223], v[68:71]\n\t"
    "ds_read_b64_tr_b16 v[150:151], v224 offset:37632\n\t"
    "s_waitcnt lgkmcnt(4)\n\t"
    "v_mfma_f32_16x16x32_bf16 v[80:83], v[136:139], a[208:211], v[80:83]\n\t"
    "ds_read_b64_tr_b16 v[152:153], v225 offset:33024\n\t"
    "v_mfma_f32_16x16x32_bf16 v[80:83], v[140:143], a[212:215], v[80:83]\n\t"
    "ds_read_b64_tr_b16 v[154:155], v225 offset:33536\n\t"
    "v_mfma_f32_16x16x32_bf16 v[84:87], v[136:139], a[216:219], v[84:87]\n\t"
    "ds_read_b64_tr_b16 v[156:157], v225 offset:37120\n\t"
    "buffer_load_dwordx4 a[116:119], v252, s[52:55], s67 offen offset:1024 sc1\n\t"
    "v_mfma_f32_16x16x32_bf16 v[84:87], v[140:143], a[220:223], v[84:87]\n\t"
    "ds_read_b64_tr_b16 v[158:159], v225 offset:37632\n\t"
    "s_waitcnt lgkmcnt(4)\n\t"
    "v_mfma_f32_16x16x32_bf16 v[96:99], v[144:147], a[208:211], v[96:99]\n\t"
    "ds_read_b64_tr_b16 v[128:129], v224 offset:49152\n\t"
    "v_mfma_f32_16x16x32_bf16 v[96:99], v[148:151], a[212:215], v[96:99]\n\t"
    "ds_read_b64_tr_b16 v[130:131], v224 offset:49664\n\t"
    "v_mfma_f32_16x16x32_bf16 v[100:103], v[144:147], a[216:219], v[100:103]\n\t"
    "ds_read_b64_tr_b16 v[132:133], v224 offset:53248\n\t"
    "buffer_load_dwordx4 a[120:123], v230, s[52:55], s67 offen offset:0 sc1\n\t"
    "v_mfma_f32_16x16x32_bf16 v[100:103], v[148:151], a[220:223], v[100:103]\n\t"
    "ds_read_b64_tr_b16 v[134:135], v224 offset:53760\n\t"
    "s_waitcnt lgkmcnt(4)\n\t"
    "v_mfma_f32_16x16x32_bf16 v[112:115], v[152:155], a[208:211], v[112:115]\n\t"
    "ds_read_b64_tr_b16 v[136:137], v225 offset:49152\n\t"
    "v_mfma_f32_16x16x32_bf16 v[112:115], v[156:159], a[212:215], v[112:115]\n\t"
    "ds_read_b64_tr_b16 v[138:139], v225 offset:49664\n\t"
    "v_mfma_f32_16x16x32_bf16 v[116:119], v[152:155], a[216:219], v[116:119]\n\t"
    "ds_read_b64_tr_b16 v[140:141], v225 offset:53248\n\t"
    "buffer_load_dwordx4 a[124:127], v230, s[52:55], s67 offen offset:1024 sc1\n\t"
    "v_mfma_f32_16x16x32_bf16 v[116:119], v[156:159], a[220:223], v[116:119]\n\t"
    "ds_read_b64_tr_b16 v[142:143], v225 offset:53760\n\t"
    "s_cmp_lg_u32 s68, 0\n\t"
    "s_cbranch_scc1 X2%=\n\t"
    "s_waitcnt vmcnt(11)\n\t"
    "v_cmp_ne_u32 vcc, v250, v249\n\t"
    "s_nop 0\n\t"
    "s_and_b32 s71, vcc_lo, 4294901760\n\t"
    "s_cmp_eq_u32 s71, 4294901760\n\t"
    "s_cbranch_scc0 SI%=\n\t"
    "RI%=:\n\t"
    "v_cmp_ne_u32 vcc, v250, v249\n\t"
    "s_nop 0\n\t"
    "s_and_b32 s71, vcc_hi, 1\n\t"
    "s_cmp_eq_u32 s71, 1\n\t"
    "s_cbranch_scc0 SP%=\n\t"
    "RP%=:\n\t"
    "X2%=:\n\t"
    "s_barrier\n\t"
    "s_waitcnt vmcnt(24) lgkmcnt(4)\n\t"
    "v_mfma_f32_16x16x32_bf16 v[0:3], v[128:131], a[192:195], v[0:3]\n\t"
    "ds_read_b64_tr_b16 v[144:145], v224 offset:49408\n\t"
    "v_mfma_f32_16x16x32_bf16 v[0:3], v[132:135], a[196:199], v[0:3]\n\t"
    "ds_read_b64_tr_b16 v[146:147], v224 offset:49920\n\t"
    "s_mov_b32 s67, 458752\n\t"
    "v_mfma_f32_16x16x32_bf16 v[4:7], v[128:131], a[200:203], v[4:7]\n\t"
    "ds_read_b64_tr_b16 v[148:149], v224 offset:53504\n\t"
    "buffer_load_dwordx4 a[240:243], v252, s[52:55], s67 offen offset:0 sc1\n\t"
    "v_mfma_f32_16x16x32_bf16 v[4:7], v[132:135], a[204:207], v[4:7]\n\t"
    "ds_read_b64_tr_b16 v[150:151], v224 offset:54016\n\t"
    "s_waitcnt lgkmcnt(4)\n\t"
    "v_mfma_f32_16x16x32_bf16 v[16:19], v[136:139], a[192:195], v[16:19]\n\t"
    "ds_read_b64_tr_b16 v[152:153], v225 offset:49408\n\t"
    "v_mfma_f32_16x16x32_bf16 v[16:19], v[140:143], a[196:199], v[16:19]\n\t"
    "ds_read_b64_tr_b16 v[154:155], v225 offset:49920\n\t"
    "v_mfma_f32_16x16x32_bf16 v[20:23], v[136:139], a[200:203], v[20:23]\n\t"
    "ds_read_b64_tr_b16 v[156:157], v225 offset:53504\n\t"
    "buffer_load_dwordx4 a[244:247], v252, s[52:55], s67 offen offset:1024 sc1\n\t"
    "v_mfma_f32_16x16x32_bf16 v[20:23], v[140:143], a[204:207], v[20:23]\n\t"
    "ds_read_b64_tr_b16 v[158:159], v225 offset:54016\n\t"
    "s_waitcnt lgkmcnt(4)\n\t"
    "v_mfma_f32_16x16x32_bf16 v[32:35], v[144:147], a[192:195], v[32:35]\n\t"
    "ds_read_b64_tr_b16 v[128:129], v226 offset:32768\n\t"
    "v_mfma_f32_16x16x32_bf16 v[32:35], v[148:151], a[196:199], v[32:35]\n\t"
    "ds_read_b64_tr_b16 v[130:131], v226 offset:33280\n\t"
    "v_mfma_f32_16x16x32_bf16 v[36:39], v[144:147], a[200:203], v[36:39]\n\t"
    "ds_read_b64_tr_b16 v[132:133], v226 offset:36864\n\t"
    "buffer_load_dwordx4 a[248:251], v230, s[52:55], s67 offen offset:0 sc1\n\t"
    "v_mfma_f32_16x16x32_bf16 v[36:39], v[148:151], a[204:207], v[36:39]\n\t"
    "ds_read_b64_tr_b16 v[134:135], v226 offset:37376\n\t"
    "s_waitcnt lgkmcnt(4)\n\t"
    "v_mfma_f32_16x16x32_bf16 v[48:51], v[152:155], a[192:195], v[48:51]\n\t"
    "ds_read_b64_tr_b16 v[136:137], v227 offset:32768\n\t"
    "v_mfma_f32_16x16x32_bf16 v[48:51], v[156:159], a[196:199], v[48:51]\n\t"
    "ds_read_b64_tr_b16 v[138:139], v227 offset:33280\n\t"
    "v_mfma_f32_16x16x32_bf16 v[52:55], v[152:155], a[200:203], v[52:55]\n\t"
    "ds_read_b64_tr_b16 v[140:141], v227 offset:36864\n\t"
    "buffer_load_dwordx4 a[252:255], v230, s[52:55], s67 offen offset:1024 sc1\n\t"
    "v_mfma_f32_16x16x32_bf16 v[52:55], v[156:159], a[204:207], v[52:55]\n\t"
    "ds_read_b64_tr_b16 v[142:143], v227 offset:37376\n\t"
    "s_waitcnt lgkmcnt(4)\n\t"
    "v_mfma_f32_16x16x32_bf16 v[64:67], v[128:131], a[192:195], v[64:67]\n\t"
    "ds_read_b64_tr_b16 v[144:145], v226 offset:33024\n\t"
    "v_mfma_f32_16x16x32_bf16 v[64:67], v[132:135], a[196:199], v[64:67]\n\t"
    "ds_read_b64_tr_b16 v[146:147], v226 offset:33536\n\t"
    "s_mov_b32 s67, 462848\n\t"
    "v_mfma_f32_16x16x32_bf16 v[68:71], v[128:131], a[200:203], v[68:71]\n\t"
    "ds_read_b64_tr_b16 v[148:149], v226 offset:37120\n\t"
    "buffer_load_dwordx4 a[224:227], v252, s[52:55], s67 offen offset:0 sc1\n\t"
    "v_mfma_f32_16x16x32_bf16 v[68:71], v[132:135], a[204:207], v[68:71]\n\t"
    "ds_read_b64_tr_b16 v[150:151], v226 offset:37632\n\t"
    "s_waitcnt lgkmcnt(4)\n\t"
    "v_mfma_f32_16x16x32_bf16 v[80:83], v[136:139], a[192:195], v[80:83]\n\t"
    "ds_read_b64_tr_b16 v[152:153], v227 offset:33024\n\t"
    "v_mfma_f32_16x16x32_bf16 v[80:83], v[140:143], a[196:199], v[80:83]\n\t"
    "ds_read_b64_tr_b16 v[154:155], v227 offset:33536\n\t"
    "v_mfma_f32_16x16x32_bf16 v[84:87], v[136:139], a[200:203], v[84:87]\n\t"
    "ds_read_b64_tr_b16 v[156:157], v227 offset:37120\n\t"
    "buffer_load_dwordx4 a[228:231], v252, s[52:55], s67 offen offset:1024 sc1\n\t"
    "v_mfma_f32_16x16x32_bf16 v[84:87], v[140:143], a[204:207], v[84:87]\n\t"
    "ds_read_b64_tr_b16 v[158:159], v227 offset:37632\n\t"
    "s_waitcnt lgkmcnt(4)\n\t"
    "v_mfma_f32_16x16x32_bf16 v[96:99], v[144:147], a[192:195], v[96:99]\n\t"
    "ds_read_b64_tr_b16 v[128:129], v226 offset:16384\n\t"
    "v_mfma_f32_16x16x32_bf16 v[96:99], v[148:151], a[196:199], v[96:99]\n\t"
    "ds_read_b64_tr_b16 v[130:131], v226 offset:16896\n\t"
    "v_mfma_f32_16x16x32_bf16 v[100:103], v[144:147], a[200:203], v[100:103]\n\t"
    "ds_read_b64_tr_b16 v[132:133], v226 offset:20480\n\t"
    "buffer_load_dwordx4 a[232:235], v230, s[52:55], s67 offen offset:0 sc1\n\t"
    "v_mfma_f32_16x16x32_bf16 v[100:103], v[148:151], a[204:207], v[100:103]\n\t"
    "ds_read_b64_tr_b16 v[134:135], v226 offset:20992\n\t"
    "s_waitcnt lgkmcnt(4)\n\t"
    "v_mfma_f32_16x16x32_bf16 v[112:115], v[152:155], a[192:195], v[112:115]\n\t"
    "ds_read_b64_tr_b16 v[136:137], v227 offset:16384\n\t"
    "v_mfma_f32_16x16x32_bf16 v[112:115], v[156:159], a[196:199], v[112:115]\n\t"
    "ds_read_b64_tr_b16 v[138:139], v227 offset:16896\n\t"
    "v_mfma_f32_16x16x32_bf16 v[116:119], v[152:155], a[200:203], v[116:119]\n\t"
    "ds_read_b64_tr_b16 v[140:141], v227 offset:20480\n\t"
    "buffer_load_dwordx4 a[236:239], v230, s[52:55], s67 offen offset:1024 sc1\n\t"
    "v_mfma_f32_16x16x32_bf16 v[116:119], v[156:159], a[204:207], v[116:119]\n\t"
    "ds_read_b64_tr_b16 v[142:143], v227 offset:20992\n\t"
    "s_waitcnt vmcnt(28) lgkmcnt(4)\n\t"
    "v_mfma_f32_16x16x32_bf16 v[0:3], v[128:131], a[32:35], v[0:3]\n\t"
    "ds_read_b64_tr_b16 v[144:145], v226 offset:16640\n\t"
    "v_mfma_f32_16x16x32_bf16 v[0:3], v[132:135], a[36:39], v[0:3]\n\t"
    "ds_read_b64_tr_b16 v[146:147], v226 offset:17152\n\t"
    "s_mov_b32 s67, 466944\n\t"
    "v_mfma_f32_16x16x32_bf16 v[4:7], v[128:131], a[40:43], v[4:7]\n\t"
    "ds_read_b64_tr_b16 v[148:149], v226 offset:20736\n\t"
    "buffer_load_dwordx4 a[208:211], v252, s[52:55], s67 offen offset:0 sc1\n\t"
    "v_mfma_f32_16x16x32_bf16 v[4:7], v[132:135], a[44:47], v[4:7]\n\t"
    "ds_read_b64_tr_b16 v[150:151], v226 offset:21248\n\t"
    "s_waitcnt lgkmcnt(4)\n\t"
    "v_mfma_f32_16x16x32_bf16 v[16:19], v[136:139], a[32:35], v[16:19]\n\t"
    "ds_read_b64_tr_b16 v[152:153], v227 offset:16640\n\t"
    "v_mfma_f32_16x16x32_bf16 v[16:19], v[140:143], a[36:39], v[16:19]\n\t"
    "ds_read_b64_tr_b16 v[154:155], v227 offset:17152\n\t"
    "v_mfma_f32_16x16x32_bf16 v[20:23], v[136:139], a[40:43], v[20:23]\n\t"
    "ds_read_b64_tr_b16 v[156:157], v227 offset:20736\n\t"
    "buffer_load_dwordx4 a[212:215], v252, s[52:55], s67 offen offset:1024 sc1\n\t"
    "v_mfma_f32_16x16x32_bf16 v[20:23], v[140:143], a[44:47], v[20:23]\n\t"
    "ds_read_b64_tr_b16 v[158:159], v227 offset:21248\n\t"
    "s_waitcnt lgkmcnt(4)\n\t"
    "v_mfma_f32_16x16x32_bf16 v[32:35], v[144:147], a[32:35], v[32:35]\n\t"
    "ds_read_b64_tr_b16 v[128:129], v226 offset:49152\n\t"
    "v_mfma_f32_16x16x32_bf16 v[32:35], v[148:151], a[36:39], v[32:35]\n\t"
    "ds_read_b64_tr_b16 v[130:131], v226 offset:49664\n\t"
    "v_mfma_f32_16x16x32_bf16 v[36:39], v[144:147], a[40:43], v[36:39]\n\t"
    "ST4_%=:\n\t"
    "s_cbranch_execz ST8_%=\n\t"
    "ds_read_b64_tr_b16 v[132:133], v226 offset:53248\n\t"
    "buffer_load_dwordx4 a[216:219], v230, s[52:55], s67 offen offset:0 sc1\n\t"
    "v_mfma_f32_16x16x32_bf16 v[36:39], v[148:151], a[44:47], v[36:39]\n\t"
    "ds_read_b64_tr_b16 v[134:135], v226 offset:53760\n\t"
    "s_waitcnt lgkmcnt(4)\n\t"
    "v_mfma_f32_16x16x32_bf16 v[48:51], v[152:155], a[32:35], v[48:51]\n\t"
    "ds_read_b64_tr_b16 v[136:137], v227 offset:49152\n\t"
    "v_mfma_f32_16x16x32_bf16 v[48:51], v[156:159], a[36:39], v[48:51]\n\t"
    "ds_read_b64_tr_b16 v[138:139], v227 offset:49664\n\t"
    "v_mfma_f32_16x16x32_bf16 v[52:55], v[152:155], a[40:43], v[52:55]\n\t"
    "ds_read_b64_tr_b16 v[140:141], v227 offset:53248\n\t"
    "buffer_load_dwordx4 a[220:223], v230, s[52:55], s67 offen offset:1024 sc1\n\t"
    "v_mfma_f32_16x16x32_bf16 v[52:55], v[156:159], a[44:47], v[52:55]\n\t"
    "ds_read_b64_tr_b16 v[142:143], v227 offset:53760\n\t"
    "s_waitcnt lgkmcnt(4)\n\t"
    "v_mfma_f32_16x16x32_bf16 v[64:67], v[128:131], a[32:35], v[64:67]\n\t"
    "ds_read_b64_tr_b16 v[144:145], v226 offset:49408\n\t"
    "v_mfma_f32_16x16x32_bf16 v[64:67], v[132:135], a[36:39], v[64:67]\n\t"
    "ds_read_b64_tr_b16 v[146:147], v226 offset:49920\n\t"
    "s_mov_b32 s67, 471040\n\t"
    "v_mfma_f32_16x16x32_bf16 v[68:71], v[128:131], a[40:43], v[68:71]\n\t"
    "ds_read_b64_tr_b16 v[148:149], v226 offset:53504\n\t"
    "buffer_load_dwordx4 a[192:195], v252, s[52:55], s67 offen offset:0 sc1\n\t"
    "v_mfma_f32_16x16x32_bf16 v[68:71], v[132:135], a[44:47], v[68:71]\n\t"
    "ds_read_b64_tr_b16 v[150:151], v226 offset:54016\n\t"
    "s_waitcnt lgkmcnt(4)\n\t"
    "v_mfma_f32_16x16x32_bf16 v[80:83], v[136:139], a[32:35], v[80:83]\n\t"
    "ds_read_b64_tr_b16 v[152:153], v227 offset:49408\n\t"
    "v_mfma_f32_16x16x32_bf16 v[80:83], v[140:143], a[36:39], v[80:83]\n\t"
    "ds_read_b64_tr_b16 v[154:155], v227 offset:49920\n\t"
    "v_mfma_f32_16x16x32_bf16 v[84:87], v[136:139], a[40:43], v[84:87]\n\t"
    "ds_read_b64_tr_b16 v[156:157], v227 offset:53504\n\t"
    "buffer_load_dwordx4 a[196:199], v252, s[52:55], s67 offen offset:1024 sc1\n\t"
    "v_mfma_f32_16x16x32_bf16 v[84:87], v[140:143], a[44:47], v[84:87]\n\t"
    "ds_read_b64_tr_b16 v[158:159], v227 offset:54016\n\t"
    "s_waitcnt lgkmcnt(4)\n\t"
    "v_mfma_f32_16x16x32_bf16 v[96:99], v[144:147], a[32:35], v[96:99]\n\t"
    "ds_read_b64_tr_b16 v[128:129], v228 offset:0\n\t"
    "v_mfma_f32_16x16x32_bf16 v[96:99], v[148:151], a[36:39], v[96:99]\n\t"
    "ds_read_b64_tr_b16 v[130:131], v228 offset:512\n\t"
    "v_mfma_f32_16x16x32_bf16 v[100:103], v[144:147], a[40:43], v[100:103]\n\t"
    "ds_read_b64_tr_b16 v[132:133], v228 offset:4096\n\t"
    "buffer_load_dwordx4 a[200:203], v230, s[52:55], s67 offen offset:0 sc1\n\t"
    "v_mfma_f32_16x16x32_bf16 v[100:103], v[148:151], a[44:47], v[100:103]\n\t"
    "ds_read_b64_tr_b16 v[134:135], v228 offset:4608\n\t"
    "s_waitcnt lgkmcnt(4)\n\t"
    "v_mfma_f32_16x16x32_bf16 v[112:115], v[152:155], a[32:35], v[112:115]\n\t"
    "ds_read_b64_tr_b16 v[136:137], v229 offset:0\n\t"
    "v_mfma_f32_16x16x32_bf16 v[112:115], v[156:159], a[36:39], v[112:115]\n\t"
    "ds_read_b64_tr_b16 v[138:139], v229 offset:512\n\t"
    "v_mfma_f32_16x16x32_bf16 v[116:119], v[152:155], a[40:43], v[116:119]\n\t"
    "ds_read_b64_tr_b16 v[140:141], v229 offset:4096\n\t"
    "buffer_load_dwordx4 a[204:207], v230, s[52:55], s67 offen offset:1024 sc1\n\t"
    "v_mfma_f32_16x16x32_bf16 v[116:119], v[156:159], a[44:47], v[116:119]\n\t"
    "ds_read_b64_tr_b16 v[142:143], v229 offset:4608\n\t"
    "s_waitcnt vmcnt(12) lgkmcnt(4)\n\t"
    "v_mfma_f32_16x16x32_bf16 v[8:11], v[128:131], a[240:243], 0\n\t"
    "ds_read_b64_tr_b16 v[144:145], v228 offset:256\n\t"
    "v_mfma_f32_16x16x32_bf16 v[8:11], v[132:135], a[244:247], v[8:11]\n\t"
    "ds_read_b64_tr_b16 v[146:147], v228 offset:768\n\t"
    "s_mov_b32 s67, 475136\n\t"
    "v_mfma_f32_16x16x32_bf16 v[12:15], v[128:131], a[248:251], 0\n\t"
    "ds_read_b64_tr_b16 v[148:149], v228 offset:4352\n\t"
    "buffer_load_dwordx4 a[32:35], v252, s[52:55], s67 offen offset:0 sc1\n\t"
    "v_mfma_f32_16x16x32_bf16 v[12:15], v[132:135], a[252:255], v[12:15]\n\t"
    "ds_read_b64_tr_b16 v[150:151], v228 offset:4864\n\t"
    "s_waitcnt lgkmcnt(4)\n\t"
    "v_mfma_f32_16x16x32_bf16 v[24:27], v[136:139], a[240:243], 0\n\t"
    "ds_read_b64_tr_b16 v[152:153], v229 offset:256\n\t"
    "v_mfma_f32_16x16x32_bf16 v[24:27], v[140:143], a[244:247], v[24:27]\n\t"
    "ds_read_b64_tr_b16 v[154:155], v229 offset:768\n\t"
    "v_mfma_f32_16x16x32_bf16 v[28:31], v[136:139], a[248:251], 0\n\t"
    "ds_read_b64_tr_b16 v[156:157], v229 offset:4352\n\t"
    "buffer_load_dwordx4 a[36:39], v252, s[52:55], s67 offen offset:1024 sc1\n\t"
    "v_mfma_f32_16x16x32_bf16 v[28:31], v[140:143], a[252:255], v[28:31]\n\t"
    "ds_read_b64_tr_b16 v[158:159], v229 offset:4864\n\t"
    "s_waitcnt lgkmcnt(4)\n\t"
    "v_mfma_f32_16x16x32_bf16 v[40:43], v[144:147], a[240:243], 0\n\t"
    "ds_read_b64_tr_b16 v[128:129], v228 offset:16384\n\t"
    "v_mfma_f32_16x16x32_bf16 v[40:43], v[148:151], a[244:247], v[40:43]\n\t"
    "ds_read_b64_tr_b16 v[130:131], v228 offset:16896\n\t"
    "v_mfma_f32_16x16x32_bf16 v[44:47], v[144:147], a[248:251], 0\n\t"
    "ds_read_b64_tr_b16 v[132:133], v228 offset:20480\n\t"
    "buffer_load_dwordx4 a[40:43], v230, s[52:55], s67 offen offset:0 sc1\n\t"
    "v_mfma_f32_16x16x32_bf16 v[44:47], v[148:151], a[252:255], v[44:47]\n\t"
    "ds_read_b64_tr_b16 v[134:135], v228 offset:20992\n\t"
    "s_waitcnt lgkmcnt(4)\n\t"
    "v_mfma_f32_16x16x32_bf16 v[56:59], v[152:155], a[240:243], 0\n\t"
    "ds_read_b64_tr_b16 v[136:137], v229 offset:16384\n\t"
    "v_mfma_f32_16x16x32_bf16 v[56:59], v[156:159], a[244:247], v[56:59]\n\t"
    "ds_read_b64_tr_b16 v[138:139], v229 offset:16896\n\t"
    "v_mfma_f32_16x16x32_bf16 v[60:63], v[152:155], a[248:251], 0\n\t"
    "ds_read_b64_tr_b16 v[140:141], v229 offset:20480\n\t"
    "buffer_load_dwordx4 a[44:47], v230, s[52:55], s67 offen offset:1024 sc1\n\t"
    "v_mfma_f32_16x16x32_bf16 v[60:63], v[156:159], a[252:255], v[60:63]\n\t"
    "ds_read_b64_tr_b16 v[142:143], v229 offset:20992\n\t"
    "s_waitcnt lgkmcnt(4)\n\t"
    "v_mfma_f32_16x16x32_bf16 v[72:75], v[128:131], a[240:243], 0\n\t"
    "ds_read_b64_tr_b16 v[144:145], v228 offset:16640\n\t"
    "v_mfma_f32_16x16x32_bf16 v[72:75], v[132:135], a[244:247], v[72:75]\n\t"
    "ds_read_b64_tr_b16 v[146:147], v228 offset:17152\n\t"
    "s_mov_b32 s67, 479232\n\t"
    "v_mfma_f32_16x16x32_bf16 v[76:79], v[128:131], a[248:251], 0\n\t"
    "ds_read_b64_tr_b16 v[148:149], v228 offset:20736\n\t"
    "buffer_load_dwordx4 a[16:19], v252, s[52:55], s67 offen offset:0 sc1\n\t"
    "v_mfma_f32_16x16x32_bf16 v[76:79], v[132:135], a[252:255], v[76:79]\n\t"
    "ds_read_b64_tr_b16 v[150:151], v228 offset:21248\n\t"
    "s_waitcnt lgkmcnt(4)\n\t"
    "v_mfma_f32_16x16x32_bf16 v[88:91], v[136:139], a[240:243], 0\n\t"
    "ds_read_b64_tr_b16 v[152:153], v229 offset:16640\n\t"
    "v_mfma_f32_16x16x32_bf16 v[88:91], v[140:143], a[244:247], v[88:91]\n\t"
    "ds_read_b64_tr_b16 v[154:155], v229 offset:17152\n\t"
    "v_mfma_f32_16x16x32_bf16 v[92:95], v[136:139], a[248:251], 0\n\t"
    "ds_read_b64_tr_b16 v[156:157], v229 offset:20736\n\t"
    "buffer_load_dwordx4 a[20:23], v252, s[52:55], s67 offen offset:1024 sc1\n\t"
    "v_mfma_f32_16x16x32_bf16 v[92:95], v[140:143], a[252:255], v[92:95]\n\t"
    "ds_read_b64_tr_b16 v[158:159], v229 offset:21248\n\t"
    "s_waitcnt lgkmcnt(4)\n\t"
    "v_mfma_f32_16x16x32_bf16 v[104:107], v[144:147], a[240:243], 0\n\t"
    "ds_read_b64_tr_b16 v[128:129], v224 offset:0\n\t"
    "v_mfma_f32_16x16x32_bf16 v[104:107], v[148:151], a[244:247], v[104:107]\n\t"
    "ds_read_b64_tr_b16 v[130:131], v224 offset:512\n\t"
    "v_mfma_f32_16x16x32_bf16 v[108:111], v[144:147], a[248:251], 0\n\t"
    "ds_read_b64_tr_b16 v[132:133], v224 offset:4096\n\t"
    "buffer_load_dwordx4 a[24:27], v230, s[52:55], s67 offen offset:0 sc1\n\t"
    "v_mfma_f32_16x16x32_bf16 v[108:111], v[148:151], a[252:255], v[108:111]\n\t"
    "ds_read_b64_tr_b16 v[134:135], v224 offset:4608\n\t"
    "s_waitcnt lgkmcnt(4)\n\t"
    "v_mfma_f32_16x16x32_bf16 v[120:123], v[152:155], a[240:243], 0\n\t"
    "ds_read_b64_tr_b16 v[136:137], v225 offset:0\n\t"
    "v_mfma_f32_16x16x32_bf16 v[120:123], v[156:159], a[244:247], v[120:123]\n\t"
    "ds_read_b64_tr_b16 v[138:139], v225 offset:512\n\t"
    "v_mfma_f32_16x16x32_bf16 v[124:127], v[152:155], a[248:251], 0\n\t"
    "ds_read_b64_tr_b16 v[140:141], v225 offset:4096\n\t"
    "buffer_load_dwordx4 a[28:31], v230, s[52:55], s67 offen offset:1024 sc1\n\t"
    "v_mfma_f32_16x16x32_bf16 v[124:127], v[156:159], a[252:255], v[124:127]\n\t"
    "ds_read_b64_tr_b16 v[142:143], v225 offset:4608\n\t"
    "s_barrier\n\t"
    "s_waitcnt vmcnt(16) lgkmcnt(4)\n\t"
    "v_mfma_f32_16x16x32_bf16 v[8:11], v[128:131], a[224:227], v[8:11]\n\t"
    "ds_read_b64_tr_b16 v[144:145], v224 offset:256\n\t"
    "s_mov_b32 s66, 1310720\n\t"
    "v_mfma_f32_16x16x32_bf16 v[8:11], v[132:135], a[228:231], v[8:11]\n\t"
    "ds_read_b64_tr_b16 v[146:147], v224 offset:768\n\t"
    "s_add_u32 m0, s64, 131072\n\t"
    "v_mfma_f32_16x16x32_bf16 v[12:15], v[128:131], a[232:235], v[12:15]\n\t"
    "ds_read_b64_tr_b16 v[148:149], v224 offset:4352\n\t"
    "buffer_load_dwordx4 v246, s[48:51], s66 offen lds\n\t"
    "v_mfma_f32_16x16x32_bf16 v[12:15], v[132:135], a[236:239], v[12:15]\n\t"
    "ds_read_b64_tr_b16 v[150:151], v224 offset:4864\n\t"
    "s_waitcnt lgkmcnt(4)\n\t"
    "v_mfma_f32_16x16x32_bf16 v[24:27], v[136:139], a[224:227], v[24:27]\n\t"
    "ds_read_b64_tr_b16 v[152:153], v225 offset:256\n\t"
    "s_mov_b32 s66, 1376256\n\t"
    "v_mfma_f32_16x16x32_bf16 v[24:27], v[140:143], a[228:231], v[24:27]\n\t"
    "ds_read_b64_tr_b16 v[154:155], v225 offset:768\n\t"
    "s_add_u32 m0, s64, 135168\n\t"
    "v_mfma_f32_16x16x32_bf16 v[28:31], v[136:139], a[232:235], v[28:31]\n\t"
    "ds_read_b64_tr_b16 v[156:157], v225 offset:4352\n\t"
    "buffer_load_dwordx4 v246, s[48:51], s66 offen lds\n\t"
    "v_mfma_f32_16x16x32_bf16 v[28:31], v[140:143], a[236:239], v[28:31]\n\t"
    "ds_read_b64_tr_b16 v[158:159], v225 offset:4864\n\t"
    "s_waitcnt lgkmcnt(4)\n\t"
    "v_mfma_f32_16x16x32_bf16 v[40:43], v[144:147], a[224:227], v[40:43]\n\t"
    "ds_read_b64_tr_b16 v[128:129], v224 offset:16384\n\t"
    "s_mov_b32 s66, 1441792\n\t"
    "v_mfma_f32_16x16x32_bf16 v[40:43], v[148:151], a[228:231], v[40:43]\n\t"
    "ds_read_b64_tr_b16 v[130:131], v224 offset:16896\n\t"
    "s_add_u32 m0, s64, 139264\n\t"
    "v_mfma_f32_16x16x32_bf16 v[44:47], v[144:147], a[232:235], v[44:47]\n\t"
    "ds_read_b64_tr_b16 v[132:133], v224 offset:20480\n\t"
    "buffer_load_dwordx4 v232, s[48:51], s66 offen lds\n\t"
    "v_mfma_f32_16x16x32_bf16 v[44:47], v[148:151], a[236:239], v[44:47]\n\t"
    "ds_read_b64_tr_b16 v[134:135], v224 offset:20992\n\t"
    "s_waitcnt lgkmcnt(4)\n\t"
    "v_mfma_f32_16x16x32_bf16 v[56:59], v[152:155], a[224:227], v[56:59]\n\t"
    "ds_read_b64_tr_b16 v[136:137], v225 offset:16384\n\t"
    "s_mov_b32 s66, 1507328\n\t"
    "v_mfma_f32_16x16x32_bf16 v[56:59], v[156:159], a[228:231], v[56:59]\n\t"
    "ds_read_b64_tr_b16 v[138:139], v225 offset:16896\n\t"
    "s_add_u32 m0, s64, 143360\n\t"
    "v_mfma_f32_16x16x32_bf16 v[60:63], v[152:155], a[232:235], v[60:63]\n\t"
    "ds_read_b64_tr_b16 v[140:141], v225 offset:20480\n\t"
    "buffer_load_dwordx4 v232, s[48:51], s66 offen lds\n\t"
    "v_mfma_f32_16x16x32_bf16 v[60:63], v[156:159], a[236:239], v[60:63]\n\t"
    "ds_read_b64_tr_b16 v[142:143], v225 offset:20992\n\t"
    "s_waitcnt lgkmcnt(4)\n\t"
    "v_mfma_f32_16x16x32_bf16 v[72:75], v[128:131], a[224:227], v[72:75]\n\t"
    "ds_read_b64_tr_b16 v[144:145], v224 offset:16640\n\t"
    "s_mov_b32 s66, 1310720\n\t"
    "v_mfma_f32_16x16x32_bf16 v[72:75], v[132:135], a[228:231], v[72:75]\n\t"
    "ds_read_b64_tr_b16 v[146:147], v224 offset:17152\n\t"
    "s_add_u32 m0, s64, 147456\n\t"
    "v_mfma_f32_16x16x32_bf16 v[76:79], v[128:131], a[232:235], v[76:79]\n\t"
    "ds_read_b64_tr_b16 v[148:149], v224 offset:20736\n\t"
    "buffer_load_dwordx4 v247, s[48:51], s66 offen lds\n\t"
    "v_mfma_f32_16x16x32_bf16 v[76:79], v[132:135], a[236:239], v[76:79]\n\t"
    "ds_read_b64_tr_b16 v[150:151], v224 offset:21248\n\t"
    "s_waitcnt lgkmcnt(4)\n\t"
    "v_mfma_f32_16x16x32_bf16 v[88:91], v[136:139], a[224:227], v[88:91]\n\t"
    "ds_read_b64_tr_b16 v[152:153], v225 offset:16640\n\t"
    "s_mov_b32 s66, 1376256\n\t"
    "v_mfma_f32_16x16x32_bf16 v[88:91], v[140:143], a[228:231], v[88:91]\n\t"
    "ds_read_b64_tr_b16 v[154:155], v225 offset:17152\n\t"
    "s_add_u32 m0, s64, 151552\n\t"
    "v_mfma_f32_16x16x32_bf16 v[92:95], v[136:139], a[232:235], v[92:95]\n\t"
    "ds_read_b64_tr_b16 v[156:157], v225 offset:20736\n\t"
    "buffer_load_dwordx4 v247, s[48:51], s66 offen lds\n\t"
    "v_mfma_f32_16x16x32_bf16 v[92:95], v[140:143], a[236:239], v[92:95]\n\t"
    "ds_read_b64_tr_b16 v[158:159], v225 offset:21248\n\t"
    "s_waitcnt lgkmcnt(4)\n\t"
    "v_mfma_f32_16x16x32_bf16 v[104:107], v[144:147], a[224:227], v[104:107]\n\t"
    "ds_read_b64_tr_b16 v[128:129], v226 offset:0\n\t"
    "s_mov_b32 s66, 1441792\n\t"
    "v_mfma_f32_16x16x32_bf16 v[104:107], v[148:151], a[228:231], v[104:107]\n\t"
    "ds_read_b64_tr_b16 v[130:131], v226 offset:512\n\t"
    "s_add_u32 m0, s64, 155648\n\t"
    "v_mfma_f32_16x16x32_bf16 v[108:111], v[144:147], a[232:235], v[108:111]\n\t"
    "ds_read_b64_tr_b16 v[132:133], v226 offset:4096\n\t"
    "buffer_load_dwordx4 v233, s[48:51], s66 offen lds\n\t"
    "v_mfma_f32_16x16x32_bf16 v[108:111], v[148:151], a[236:239], v[108:111]\n\t"
    "ds_read_b64_tr_b16 v[134:135], v226 offset:4608\n\t"
    "s_waitcnt lgkmcnt(4)\n\t"
    "v_mfma_f32_16x16x32_bf16 v[120:123], v[152:155], a[224:227], v[120:123]\n\t"
    "ds_read_b64_tr_b16 v[136:137], v227 offset:0\n\t"
    "s_mov_b32 s66, 1507328\n\t"
    "v_mfma_f32_16x16x32_bf16 v[120:123], v[156:159], a[228:231], v[120:123]\n\t"
    "ds_read_b64_tr_b16 v[138:139], v227 offset:512\n\t"
    "s_add_u32 m0, s64, 159744\n\t"
    "v_mfma_f32_16x16x32_bf16 v[124:127], v[152:155], a[232:235], v[124:127]\n\t"
    "ds_read_b64_tr_b16 v[140:141], v227 offset:4096\n\t"
    "buffer_load_dwordx4 v233, s[48:51], s66 offen lds\n\t"
    "v_mfma_f32_16x16x32_bf16 v[124:127], v[156:159], a[236:239], v[124:127]\n\t"
    "ds_read_b64_tr_b16 v[142:143], v227 offset:4608\n\t"
    "s_waitcnt vmcnt(20) lgkmcnt(4)\n\t"
    "v_mfma_f32_16x16x32_bf16 v[8:11], v[128:131], a[208:211], v[8:11]\n\t"
    "ds_read_b64_tr_b16 v[144:145], v226 offset:256\n\t"
    "v_mfma_f32_16x16x32_bf16 v[8:11], v[132:135], a[212:215], v[8:11]\n\t"
    "ds_read_b64_tr_b16 v[146:147], v226 offset:768\n\t"
    "s_mov_b32 s67, 483328\n\t"
    "v_mfma_f32_16x16x32_bf16 v[12:15], v[128:131], a[216:219], v[12:15]\n\t"
    "ds_read_b64_tr_b16 v[148:149], v226 offset:4352\n\t"
    "buffer_load_dwordx4 a[0:3], v252, s[52:55], s67 offen offset:0 sc1\n\t"
    "v_mfma_f32_16x16x32_bf16 v[12:15], v[132:135], a[220:223], v[12:15]\n\t"
    "ds_read_b64_tr_b16 v[150:151], v226 offset:4864\n\t"
    "s_waitcnt lgkmcnt(4)\n\t"
    "v_mfma_f32_16x16x32_bf16 v[24:27], v[136:139], a[208:211], v[24:27]\n\t"
    "ds_read_b64_tr_b16 v[152:153], v227 offset:256\n\t"
    "v_mfma_f32_16x16x32_bf16 v[24:27], v[140:143], a[212:215], v[24:27]\n\t"
    "ds_read_b64_tr_b16 v[154:155], v227 offset:768\n\t"
    "v_mfma_f32_16x16x32_bf16 v[28:31], v[136:139], a[216:219], v[28:31]\n\t"
    "ds_read_b64_tr_b16 v[156:157], v227 offset:4352\n\t"
    "buffer_load_dwordx4 a[4:7], v252, s[52:55], s67 offen offset:1024 sc1\n\t"
    "v_mfma_f32_16x16x32_bf16 v[28:31], v[140:143], a[220:223], v[28:31]\n\t"
    "ds_read_b64_tr_b16 v[158:159], v227 offset:4864\n\t"
    "s_waitcnt lgkmcnt(4)\n\t"
    "v_mfma_f32_16x16x32_bf16 v[40:43], v[144:147], a[208:211], v[40:43]\n\t"
    "ds_read_b64_tr_b16 v[128:129], v224 offset:32768\n\t"
    "v_mfma_f32_16x16x32_bf16 v[40:43], v[148:151], a[212:215], v[40:43]\n\t"
    "ds_read_b64_tr_b16 v[130:131], v224 offset:33280\n\t"
    "v_mfma_f32_16x16x32_bf16 v[44:47], v[144:147], a[216:219], v[44:47]\n\t"
    "ds_read_b64_tr_b16 v[132:133], v224 offset:36864\n\t"
    "buffer_load_dwordx4 a[8:11], v230, s[52:55], s67 offen offset:0 sc1\n\t"
    "v_mfma_f32_16x16x32_bf16 v[44:47], v[148:151], a[220:223], v[44:47]\n\t"
    "ds_read_b64_tr_b16 v[134:135], v224 offset:37376\n\t"
    "s_waitcnt lgkmcnt(4)\n\t"
    "v_mfma_f32_16x16x32_bf16 v[56:59], v[152:155], a[208:211], v[56:59]\n\t"
    "ds_read_b64_tr_b16 v[136:137], v225 offset:32768\n\t"
    "v_mfma_f32_16x16x32_bf16 v[56:59], v[156:159], a[212:215], v[56:59]\n\t"
    "ds_read_b64_tr_b16 v[138:139], v225 offset:33280\n\t"
    "v_mfma_f32_16x16x32_bf16 v[60:63], v[152:155], a[216:219], v[60:63]\n\t"
    "ds_read_b64_tr_b16 v[140:141], v225 offset:36864\n\t"
    "buffer_load_dwordx4 a[12:15], v230, s[52:55], s67 offen offset:1024 sc1\n\t"
    "v_mfma_f32_16x16x32_bf16 v[60:63], v[156:159], a[220:223], v[60:63]\n\t"
    "ds_read_b64_tr_b16 v[142:143], v225 offset:37376\n\t"
    "s_waitcnt lgkmcnt(4)\n\t"
    "v_mfma_f32_16x16x32_bf16 v[72:75], v[128:131], a[208:211], v[72:75]\n\t"
    "ds_read_b64_tr_b16 v[144:145], v224 offset:33024\n\t"
    "v_mfma_f32_16x16x32_bf16 v[72:75], v[132:135], a[212:215], v[72:75]\n\t"
    "ds_read_b64_tr_b16 v[146:147], v224 offset:33536\n\t"
    "s_mov_b32 s67, 487424\n\t"
    "v_mfma_f32_16x16x32_bf16 v[76:79], v[128:131], a[216:219], v[76:79]\n\t"
    "ds_read_b64_tr_b16 v[148:149], v224 offset:37120\n\t"
    "buffer_load_dwordx4 v[160:163], v252, s[52:55], s67 offen offset:0 sc1\n\t"
    "v_mfma_f32_16x16x32_bf16 v[76:79], v[132:135], a[220:223], v[76:79]\n\t"
    "ds_read_b64_tr_b16 v[150:151], v224 offset:37632\n\t"
    "s_waitcnt lgkmcnt(4)\n\t"
    "v_mfma_f32_16x16x32_bf16 v[88:91], v[136:139], a[208:211], v[88:91]\n\t"
    "ds_read_b64_tr_b16 v[152:153], v225 offset:33024\n\t"
    "v_mfma_f32_16x16x32_bf16 v[88:91], v[140:143], a[212:215], v[88:91]\n\t"
    "ds_read_b64_tr_b16 v[154:155], v225 offset:33536\n\t"
    "v_mfma_f32_16x16x32_bf16 v[92:95], v[136:139], a[216:219], v[92:95]\n\t"
    "ds_read_b64_tr_b16 v[156:157], v225 offset:37120\n\t"
    "buffer_load_dwordx4 v[164:167], v252, s[52:55], s67 offen offset:1024 sc1\n\t"
    "v_mfma_f32_16x16x32_bf16 v[92:95], v[140:143], a[220:223], v[92:95]\n\t"
    "ds_read_b64_tr_b16 v[158:159], v225 offset:37632\n\t"
    "s_waitcnt lgkmcnt(4)\n\t"
    "v_mfma_f32_16x16x32_bf16 v[104:107], v[144:147], a[208:211], v[104:107]\n\t"
    "ds_read_b64_tr_b16 v[128:129], v224 offset:49152\n\t"
    "v_mfma_f32_16x16x32_bf16 v[104:107], v[148:151], a[212:215], v[104:107]\n\t"
    "ds_read_b64_tr_b16 v[130:131], v224 offset:49664\n\t"
    "v_mfma_f32_16x16x32_bf16 v[108:111], v[144:147], a[216:219], v[108:111]\n\t"
    "ds_read_b64_tr_b16 v[132:133], v224 offset:53248\n\t"
    "buffer_load_dwordx4 v[168:171], v230, s[52:55], s67 offen offset:0 sc1\n\t"
    "v_mfma_f32_16x16x32_bf16 v[108:111], v[148:151], a[220:223], v[108:111]\n\t"
    "ds_read_b64_tr_b16 v[134:135], v224 offset:53760\n\t"
    "s_waitcnt lgkmcnt(4)\n\t"
    "v_mfma_f32_16x16x32_bf16 v[120:123], v[152:155], a[208:211], v[120:123]\n\t"
    "ds_read_b64_tr_b16 v[136:137], v225 offset:49152\n\t"
    "v_mfma_f32_16x16x32_bf16 v[120:123], v[156:159], a[212:215], v[120:123]\n\t"
    "ds_read_b64_tr_b16 v[138:139], v225 offset:49664\n\t"
    "v_mfma_f32_16x16x32_bf16 v[124:127], v[152:155], a[216:219], v[124:127]\n\t"
    "ds_read_b64_tr_b16 v[140:141], v225 offset:53248\n\t"
    "buffer_load_dwordx4 v[172:175], v230, s[52:55], s67 offen offset:1024 sc1\n\t"
    "v_mfma_f32_16x16x32_bf16 v[124:127], v[156:159], a[220:223], v[124:127]\n\t"
    "ds_read_b64_tr_b16 v[142:143], v225 offset:53760\n\t"
    "s_barrier\n\t"
    "s_waitcnt vmcnt(24) lgkmcnt(4)\n\t"
    "v_mfma_f32_16x16x32_bf16 v[8:11], v[128:131], a[192:195], v[8:11]\n\t"
    "ds_read_b64_tr_b16 v[144:145], v224 offset:49408\n\t"
    "s_mov_b32 s66, 1572864\n\t"
    "v_mfma_f32_16x16x32_bf16 v[8:11], v[132:135], a[196:199], v[8:11]\n\t"
    "ds_read_b64_tr_b16 v[146:147], v224 offset:49920\n\t"
    "s_add_u32 m0, s64, 0\n\t"
    "v_mfma_f32_16x16x32_bf16 v[12:15], v[128:131], a[200:203], v[12:15]\n\t"
    "ds_read_b64_tr_b16 v[148:149], v224 offset:53504\n\t"
    "buffer_load_dwordx4 v246, s[48:51], s66 offen lds\n\t"
    "v_mfma_f32_16x16x32_bf16 v[12:15], v[132:135], a[204:207], v[12:15]\n\t"
    "ds_read_b64_tr_b16 v[150:151], v224 offset:54016\n\t"
    "s_waitcnt lgkmcnt(4)\n\t"
    "v_mfma_f32_16x16x32_bf16 v[24:27], v[136:139], a[192:195], v[24:27]\n\t"
    "ds_read_b64_tr_b16 v[152:153], v225 offset:49408\n\t"
    "s_mov_b32 s66, 1638400\n\t"
    "v_mfma_f32_16x16x32_bf16 v[24:27], v[140:143], a[196:199], v[24:27]\n\t"
    "ds_read_b64_tr_b16 v[154:155], v225 offset:49920\n\t"
    "s_add_u32 m0, s64, 4096\n\t"
    "v_mfma_f32_16x16x32_bf16 v[28:31], v[136:139], a[200:203], v[28:31]\n\t"
    "ds_read_b64_tr_b16 v[156:157], v225 offset:53504\n\t"
    "buffer_load_dwordx4 v246, s[48:51], s66 offen lds\n\t"
    "v_mfma_f32_16x16x32_bf16 v[28:31], v[140:143], a[204:207], v[28:31]\n\t"
    "ds_read_b64_tr_b16 v[158:159], v225 offset:54016\n\t"
    "s_waitcnt lgkmcnt(4)\n\t"
    "v_mfma_f32_16x16x32_bf16 v[40:43], v[144:147], a[192:195], v[40:43]\n\t"
    "ds_read_b64_tr_b16 v[128:129], v226 offset:32768\n\t"
    "s_mov_b32 s66, 1703936\n\t"
    "v_mfma_f32_16x16x32_bf16 v[40:43], v[148:151], a[196:199], v[40:43]\n\t"
    "ds_read_b64_tr_b16 v[130:131], v226 offset:33280\n\t"
    "s_add_u32 m0, s64, 8192\n\t"
    "v_mfma_f32_16x16x32_bf16 v[44:47], v[144:147], a[200:203], v[44:47]\n\t"
    "ds_read_b64_tr_b16 v[132:133], v226 offset:36864\n\t"
    "buffer_load_dwordx4 v232, s[48:51], s66 offen lds\n\t"
    "v_mfma_f32_16x16x32_bf16 v[44:47], v[148:151], a[204:207], v[44:47]\n\t"
    "ds_read_b64_tr_b16 v[134:135], v226 offset:37376\n\t"
    "s_waitcnt lgkmcnt(4)\n\t"
    "v_mfma_f32_16x16x32_bf16 v[56:59], v[152:155], a[192:195], v[56:59]\n\t"
    "ds_read_b64_tr_b16 v[136:137], v227 offset:32768\n\t"
    "s_mov_b32 s66, 1769472\n\t"
    "v_mfma_f32_16x16x32_bf16 v[56:59], v[156:159], a[196:199], v[56:59]\n\t"
    "ds_read_b64_tr_b16 v[138:139], v227 offset:33280\n\t"
    "s_add_u32 m0, s64, 12288\n\t"
    "v_mfma_f32_16x16x32_bf16 v[60:63], v[152:155], a[200:203], v[60:63]\n\t"
    "ds_read_b64_tr_b16 v[140:141], v227 offset:36864\n\t"
    "buffer_load_dwordx4 v232, s[48:51], s66 offen lds\n\t"
    "v_mfma_f32_16x16x32_bf16 v[60:63], v[156:159], a[204:207], v[60:63]\n\t"
    "ds_read_b64_tr_b16 v[142:143], v227 offset:37376\n\t"
    "s_waitcnt lgkmcnt(4)\n\t"
    "v_mfma_f32_16x16x32_bf16 v[72:75], v[128:131], a[192:195], v[72:75]\n\t"
    "ds_read_b64_tr_b16 v[144:145], v226 offset:33024\n\t"
    "s_mov_b32 s66, 1835008\n\t"
    "v_mfma_f32_16x16x32_bf16 v[72:75], v[132:135], a[196:199], v[72:75]\n\t"
    "ds_read_b64_tr_b16 v[146:147], v226 offset:33536\n\t"
    "s_add_u32 m0, s64, 16384\n\t"
    "v_mfma_f32_16x16x32_bf16 v[76:79], v[128:131], a[200:203], v[76:79]\n\t"
    "ds_read_b64_tr_b16 v[148:149], v226 offset:37120\n\t"
    "buffer_load_dwordx4 v246, s[48:51], s66 offen lds\n\t"
    "v_mfma_f32_16x16x32_bf16 v[76:79], v[132:135], a[204:207], v[76:79]\n\t"
    "ds_read_b64_tr_b16 v[150:151], v226 offset:37632\n\t"
    "s_waitcnt lgkmcnt(4)\n\t"
    "v_mfma_f32_16x16x32_bf16 v[88:91], v[136:139], a[192:195], v[88:91]\n\t"
    "ds_read_b64_tr_b16 v[152:153], v227 offset:33024\n\t"
    "s_mov_b32 s66, 1900544\n\t"
    "v_mfma_f32_16x16x32_bf16 v[88:91], v[140:143], a[196:199], v[88:91]\n\t"
    "ds_read_b64_tr_b16 v[154:155], v227 offset:33536\n\t"
    "s_add_u32 m0, s64, 20480\n\t"
    "v_mfma_f32_16x16x32_bf16 v[92:95], v[136:139], a[200:203], v[92:95]\n\t"
    "ds_read_b64_tr_b16 v[156:157], v227 offset:37120\n\t"
    "buffer_load_dwordx4 v246, s[48:51], s66 offen lds\n\t"
    "v_mfma_f32_16x16x32_bf16 v[92:95], v[140:143], a[204:207], v[92:95]\n\t"
    "ds_read_b64_tr_b16 v[158:159], v227 offset:37632\n\t"
    "s_waitcnt lgkmcnt(4)\n\t"
    "v_mfma_f32_16x16x32_bf16 v[104:107], v[144:147], a[192:195], v[104:107]\n\t"
    "ds_read_b64_tr_b16 v[128:129], v226 offset:16384\n\t"
    "s_mov_b32 s66, 1966080\n\t"
    "v_mfma_f32_16x16x32_bf16 v[104:107], v[148:151], a[196:199], v[104:107]\n\t"
    "ds_read_b64_tr_b16 v[130:131], v226 offset:16896\n\t"
    "s_add_u32 m0, s64, 24576\n\t"
    "v_mfma_f32_16x16x32_bf16 v[108:111], v[144:147], a[200:203], v[108:111]\n\t"
    "ds_read_b64_tr_b16 v[132:133], v226 offset:20480\n\t"
    "buffer_load_dwordx4 v232, s[48:51], s66 offen lds\n\t"
    "v_mfma_f32_16x16x32_bf16 v[108:111], v[148:151], a[204:207], v[108:111]\n\t"
    "ds_read_b64_tr_b16 v[134:135], v226 offset:20992\n\t"
    "s_waitcnt lgkmcnt(4)\n\t"
    "v_mfma_f32_16x16x32_bf16 v[120:123], v[152:155], a[192:195], v[120:123]\n\t"
    "ds_read_b64_tr_b16 v[136:137], v227 offset:16384\n\t"
    "s_mov_b32 s66, 2031616\n\t"
    "v_mfma_f32_16x16x32_bf16 v[120:123], v[156:159], a[196:199], v[120:123]\n\t"
    "ds_read_b64_tr_b16 v[138:139], v227 offset:16896\n\t"
    "s_add_u32 m0, s64, 28672\n\t"
    "v_mfma_f32_16x16x32_bf16 v[124:127], v[152:155], a[200:203], v[124:127]\n\t"
    "ds_read_b64_tr_b16 v[140:141], v227 offset:20480\n\t"
    "buffer_load_dwordx4 v232, s[48:51], s66 offen lds\n\t"
    "v_mfma_f32_16x16x32_bf16 v[124:127], v[156:159], a[204:207], v[124:127]\n\t"
    "ds_read_b64_tr_b16 v[142:143], v227 offset:20992\n\t"
    "s_waitcnt vmcnt(28) lgkmcnt(4)\n\t"
    "v_mfma_f32_16x16x32_bf16 v[8:11], v[128:131], a[32:35], v[8:11]\n\t"
    "ds_read_b64_tr_b16 v[144:145], v226 offset:16640\n\t"
    "s_mov_b32 s66, 2097152\n\t"
    "v_mfma_f32_16x16x32_bf16 v[8:11], v[132:135], a[36:39], v[8:11]\n\t"
    "ds_read_b64_tr_b16 v[146:147], v226 offset:17152\n\t"
    "s_add_u32 m0, s64, 65536\n\t"
    "v_mfma_f32_16x16x32_bf16 v[12:15], v[128:131], a[40:43], v[12:15]\n\t"
    "ds_read_b64_tr_b16 v[148:149], v226 offset:20736\n\t"
    "buffer_load_dwordx4 v246, s[48:51], s66 offen lds\n\t"
    "v_mfma_f32_16x16x32_bf16 v[12:15], v[132:135], a[44:47], v[12:15]\n\t"
    "ds_read_b64_tr_b16 v[150:151], v226 offset:21248\n\t"
    "s_waitcnt lgkmcnt(4)\n\t"
    "v_mfma_f32_16x16x32_bf16 v[24:27], v[136:139], a[32:35], v[24:27]\n\t"
    "ds_read_b64_tr_b16 v[152:153], v227 offset:16640\n\t"
    "s_mov_b32 s66, 2162688\n\t"
    "v_mfma_f32_16x16x32_bf16 v[24:27], v[140:143], a[36:39], v[24:27]\n\t"
    "ds_read_b64_tr_b16 v[154:155], v227 offset:17152\n\t"
    "s_add_u32 m0, s64, 69632\n\t"
    "v_mfma_f32_16x16x32_bf16 v[28:31], v[136:139], a[40:43], v[28:31]\n\t"
    "ds_read_b64_tr_b16 v[156:157], v227 offset:20736\n\t"
    "buffer_load_dwordx4 v246, s[48:51], s66 offen lds\n\t"
    "v_mfma_f32_16x16x32_bf16 v[28:31], v[140:143], a[44:47], v[28:31]\n\t"
    "ds_read_b64_tr_b16 v[158:159], v227 offset:21248\n\t"
    "s_waitcnt lgkmcnt(4)\n\t"
    "v_mfma_f32_16x16x32_bf16 v[40:43], v[144:147], a[32:35], v[40:43]\n\t"
    "ds_read_b64_tr_b16 v[128:129], v226 offset:49152\n\t"
    "s_mov_b32 s66, 2228224\n\t"
    "v_mfma_f32_16x16x32_bf16 v[40:43], v[148:151], a[36:39], v[40:43]\n\t"
    "ds_read_b64_tr_b16 v[130:131], v226 offset:49664\n\t"
    "s_add_u32 m0, s64, 73728\n\t"
    "v_mfma_f32_16x16x32_bf16 v[44:47], v[144:147], a[40:43], v[44:47]\n\t"
    "ds_read_b64_tr_b16 v[132:133], v226 offset:53248\n\t"
    "buffer_load_dwordx4 v232, s[48:51], s66 offen lds\n\t"
    "v_mfma_f32_16x16x32_bf16 v[44:47], v[148:151], a[44:47], v[44:47]\n\t"
    "ds_read_b64_tr_b16 v[134:135], v226 offset:53760\n\t"
    "s_waitcnt lgkmcnt(4)\n\t"
    "v_mfma_f32_16x16x32_bf16 v[56:59], v[152:155], a[32:35], v[56:59]\n\t"
    "ds_read_b64_tr_b16 v[136:137], v227 offset:49152\n\t"
    "s_mov_b32 s66, 2293760\n\t"
    "v_mfma_f32_16x16x32_bf16 v[56:59], v[156:159], a[36:39], v[56:59]\n\t"
    "ds_read_b64_tr_b16 v[138:139], v227 offset:49664\n\t"
    "s_add_u32 m0, s64, 77824\n\t"
    "v_mfma_f32_16x16x32_bf16 v[60:63], v[152:155], a[40:43], v[60:63]\n\t"
    "ds_read_b64_tr_b16 v[140:141], v227 offset:53248\n\t"
    "buffer_load_dwordx4 v232, s[48:51], s66 offen lds\n\t"
    "v_mfma_f32_16x16x32_bf16 v[60:63], v[156:159], a[44:47], v[60:63]\n\t"
    "ds_read_b64_tr_b16 v[142:143], v227 offset:53760\n\t"
    "s_waitcnt lgkmcnt(4)\n\t"
    "v_mfma_f32_16x16x32_bf16 v[72:75], v[128:131], a[32:35], v[72:75]\n\t"
    "ds_read_b64_tr_b16 v[144:145], v226 offset:49408\n\t"
    "s_mov_b32 s66, 2359296\n\t"
    "v_mfma_f32_16x16x32_bf16 v[72:75], v[132:135], a[36:39], v[72:75]\n\t"
    "ds_read_b64_tr_b16 v[146:147], v226 offset:49920\n\t"
    "s_add_u32 m0, s64, 32768\n\t"
    "v_mfma_f32_16x16x32_bf16 v[76:79], v[128:131], a[40:43], v[76:79]\n\t"
    "ds_read_b64_tr_b16 v[148:149], v226 offset:53504\n\t"
    "buffer_load_dwordx4 v246, s[48:51], s66 offen lds\n\t"
    "v_mfma_f32_16x16x32_bf16 v[76:79], v[132:135], a[44:47], v[76:79]\n\t"
    "ds_read_b64_tr_b16 v[150:151], v226 offset:54016\n\t"
    "s_waitcnt lgkmcnt(4)\n\t"
    "v_mfma_f32_16x16x32_bf16 v[88:91], v[136:139], a[32:35], v[88:91]\n\t"
    "ds_read_b64_tr_b16 v[152:153], v227 offset:49408\n\t"
    "s_mov_b32 s66, 2424832\n\t"
    "v_mfma_f32_16x16x32_bf16 v[88:91], v[140:143], a[36:39], v[88:91]\n\t"
    "ds_read_b64_tr_b16 v[154:155], v227 offset:49920\n\t"
    "s_add_u32 m0, s64, 36864\n\t"
    "v_mfma_f32_16x16x32_bf16 v[92:95], v[136:139], a[40:43], v[92:95]\n\t"
    "ds_read_b64_tr_b16 v[156:157], v227 offset:53504\n\t"
    "buffer_load_dwordx4 v246, s[48:51], s66 offen lds\n\t"
    "v_mfma_f32_16x16x32_bf16 v[92:95], v[140:143], a[44:47], v[92:95]\n\t"
    "ds_read_b64_tr_b16 v[158:159], v227 offset:54016\n\t"
    "s_waitcnt vmcnt(22)\n\t"
    "s_barrier\n\t"
    "s_waitcnt lgkmcnt(4)\n\t"
    "v_mfma_f32_16x16x32_bf16 v[104:107], v[144:147], a[32:35], v[104:107]\n\t"
    "ds_read_b64_tr_b16 v[128:129], v228 offset:0\n\t"
    "s_mov_b32 s66, 2490368\n\t"
    "v_mfma_f32_16x16x32_bf16 v[104:107], v[148:151], a[36:39], v[104:107]\n\t"
    "ds_read_b64_tr_b16 v[130:131], v228 offset:512\n\t"
    "s_add_u32 m0, s64, 40960\n\t"
    "v_mfma_f32_16x16x32_bf16 v[108:111], v[144:147], a[40:43], v[108:111]\n\t"
    "ds_read_b64_tr_b16 v[132:133], v228 offset:4096\n\t"
    "buffer_load_dwordx4 v232, s[48:51], s66 offen lds\n\t"
    "v_mfma_f32_16x16x32_bf16 v[108:111], v[148:151], a[44:47], v[108:111]\n\t"
    "ds_read_b64_tr_b16 v[134:135], v228 offset:4608\n\t"
    "s_waitcnt lgkmcnt(4)\n\t"
    "v_mfma_f32_16x16x32_bf16 v[120:123], v[152:155], a[32:35], v[120:123]\n\t"
    "ds_read_b64_tr_b16 v[136:137], v229 offset:0\n\t"
    "s_mov_b32 s66, 2555904\n\t"
    "v_mfma_f32_16x16x32_bf16 v[120:123], v[156:159], a[36:39], v[120:123]\n\t"
    "ds_read_b64_tr_b16 v[138:139], v229 offset:512\n\t"
    "s_add_u32 m0, s64, 45056\n\t"
    "v_mfma_f32_16x16x32_bf16 v[124:127], v[152:155], a[40:43], v[124:127]\n\t"
    "ds_read_b64_tr_b16 v[140:141], v229 offset:4096\n\t"
    "buffer_load_dwordx4 v232, s[48:51], s66 offen lds\n\t"
    "v_mfma_f32_16x16x32_bf16 v[124:127], v[156:159], a[44:47], v[124:127]\n\t"
    "ds_read_b64_tr_b16 v[142:143], v229 offset:4608\n\t"
    "s_waitcnt vmcnt(63) lgkmcnt(4)\n\t"
    "v_mfma_f32_16x16x32_bf16 v[0:3], v[128:131], a[176:179], v[0:3]\n\t"
    "ds_read_b64_tr_b16 v[144:145], v228 offset:256\n\t"
    "v_mfma_f32_16x16x32_bf16 v[0:3], v[132:135], a[180:183], v[0:3]\n\t"
    "s_mov_b32 s67, 491520\n\t"
    "v_mfma_f32_16x16x32_bf16 v[4:7], v[128:131], a[184:187], v[4:7]\n\t"
    "buffer_load_dwordx4 v[176:179], v252, s[52:55], s67 offen offset:0 sc1\n\t"
    "v_mfma_f32_16x16x32_bf16 v[4:7], v[132:135], a[188:191], v[4:7]\n\t"
    "ds_read_b64_tr_b16 v[146:147], v228 offset:768\n\t"
    "s_waitcnt vmcnt(33)\n\t"
    "v_mfma_f32_16x16x32_bf16 v[8:11], v[128:131], a[16:19], v[8:11]\n\t"
    "ds_read_b64_tr_b16 v[148:149], v228 offset:4352\n\t"
    "v_mfma_f32_16x16x32_bf16 v[8:11], v[132:135], a[20:23], v[8:11]\n\t"
    "ds_read_b64_tr_b16 v[150:151], v228 offset:4864\n\t"
    "v_mfma_f32_16x16x32_bf16 v[12:15], v[128:131], a[24:27], v[12:15]\n\t"
    "buffer_load_dwordx4 v[180:183], v252, s[52:55], s67 offen offset:1024 sc1\n\t"
    "v_mfma_f32_16x16x32_bf16 v[12:15], v[132:135], a[28:31], v[12:15]\n\t"
    "s_waitcnt lgkmcnt(4)\n\t"
    "v_mfma_f32_16x16x32_bf16 v[16:19], v[136:139], a[176:179], v[16:19]\n\t"
    "ds_read_b64_tr_b16 v[152:153], v229 offset:256\n\t"
    "v_mfma_f32_16x16x32_bf16 v[16:19], v[140:143], a[180:183], v[16:19]\n\t"
    "v_mfma_f32_16x16x32_bf16 v[20:23], v[136:139], a[184:187], v[20:23]\n\t"
    "buffer_load_dwordx4 v[184:187], v230, s[52:55], s67 offen offset:0 sc1\n\t"
    "v_mfma_f32_16x16x32_bf16 v[20:23], v[140:143], a[188:191], v[20:23]\n\t"
    "ds_read_b64_tr_b16 v[154:155], v229 offset:768\n\t"
    "v_mfma_f32_16x16x32_bf16 v[24:27], v[136:139], a[16:19], v[24:27]\n\t"
    "ds_read_b64_tr_b16 v[156:157], v229 offset:4352\n\t"
    "ST5_%=:\n\t"
    "s_cbranch_execz ST9_%=\n\t"
    "s_cmp_lg_u32 s68, 0\n\t"
    "s_cbranch_scc1 H2%=\n\t"
    "s_mov_b32 exec_lo, 65535\n\t"
    "s_mov_b32 exec_hi, 1\n\t"
    "buffer_atomic_add v255, v248, s[52:55], 0 offen offset:128 sc1\n\t"
    "s_mov_b64 exec, -1\n\t"
    "H2%=:\n\t"
    "v_mfma_f32_16x16x32_bf16 v[24:27], v[140:143], a[20:23], v[24:27]\n\t"
    "ds_read_b64_tr_b16 v[158:159], v229 offset:4864\n\t"
    "v_mfma_f32_16x16x32_bf16 v[28:31], v[136:139], a[24:27], v[28:31]\n\t"
    "buffer_load_dwordx4 v[188:191], v230, s[52:55], s67 offen offset:1024 sc1\n\t"
    "v_mfma_f32_16x16x32_bf16 v[28:31], v[140:143], a[28:31], v[28:31]\n\t"
    "s_waitcnt lgkmcnt(4)\n\t"
    "v_mfma_f32_16x16x32_bf16 v[32:35], v[144:147], a[176:179], v[32:35]\n\t"
    "ds_read_b64_tr_b16 v[128:129], v228 offset:16384\n\t"
    "v_mfma_f32_16x16x32_bf16 v[32:35], v[148:151], a[180:183], v[32:35]\n\t"
    "s_mov_b32 s67, 495616\n\t"
    "v_mfma_f32_16x16x32_bf16 v[36:39], v[144:147], a[184:187], v[36:39]\n\t"
    "buffer_load_dwordx4 v[192:195], v252, s[52:55], s67 offen offset:0 sc1\n\t"
    "v_mfma_f32_16x16x32_bf16 v[36:39], v[148:151], a[188:191], v[36:39]\n\t"
    "ds_read_b64_tr_b16 v[130:131], v228 offset:16896\n\t"
    "v_mfma_f32_16x16x32_bf16 v[40:43], v[144:147], a[16:19], v[40:43]\n\t"
    "ds_read_b64_tr_b16 v[132:133], v228 offset:20480\n\t"
    "v_mfma_f32_16x16x32_bf16 v[40:43], v[148:151], a[20:23], v[40:43]\n\t"
    "ds_read_b64_tr_b16 v[134:135], v228 offset:20992\n\t"
    "v_mfma_f32_16x16x32_bf16 v[44:47], v[144:147], a[24:27], v[44:47]\n\t"
    "buffer_load_dwordx4 v[196:199], v252, s[52:55], s67 offen offset:1024 sc1\n\t"
    "v_mfma_f32_16x16x32_bf16 v[44:47], v[148:151], a[28:31], v[44:47]\n\t"
    "s_waitcnt lgkmcnt(4)\n\t"
    "v_mfma_f32_16x16x32_bf16 v[48:51], v[152:155], a[176:179], v[48:51]\n\t"
    "ds_read_b64_tr_b16 v[136:137], v229 offset:16384\n\t"
    "v_mfma_f32_16x16x32_bf16 v[48:51], v[156:159], a[180:183], v[48:51]\n\t"
    "v_mfma_f32_16x16x32_bf16 v[52:55], v[152:155], a[184:187], v[52:55]\n\t"
    "buffer_load_dwordx4 v[200:203], v230, s[52:55], s67 offen offset:0 sc1\n\t"
    "v_mfma_f32_16x16x32_bf16 v[52:55], v[156:159], a[188:191], v[52:55]\n\t"
    "ds_read_b64_tr_b16 v[138:139], v229 offset:16896\n\t"
    "v_mfma_f32_16x16x32_bf16 v[56:59], v[152:155], a[16:19], v[56:59]\n\t"
    "ds_read_b64_tr_b16 v[140:141], v229 offset:20480\n\t"
    "v_mfma_f32_16x16x32_bf16 v[56:59], v[156:159], a[20:23], v[56:59]\n\t"
    "ds_read_b64_tr_b16 v[142:143], v229 offset:20992\n\t"
    "v_mfma_f32_16x16x32_bf16 v[60:63], v[152:155], a[24:27], v[60:63]\n\t"
    "buffer_load_dwordx4 v[204:207], v230, s[52:55], s67 offen offset:1024 sc1\n\t"
    "v_mfma_f32_16x16x32_bf16 v[60:63], v[156:159], a[28:31], v[60:63]\n\t"
    "s_waitcnt lgkmcnt(4)\n\t"
    "v_mfma_f32_16x16x32_bf16 v[64:67], v[128:131], a[176:179], v[64:67]\n\t"
    "ds_read_b64_tr_b16 v[144:145], v228 offset:16640\n\t"
    "s_mov_b32 s66, 2621440\n\t"
    "v_mfma_f32_16x16x32_bf16 v[64:67], v[132:135], a[180:183], v[64:67]\n\t"
    "s_add_u32 m0, s64, 49152\n\t"
    "v_mfma_f32_16x16x32_bf16 v[68:71], v[128:131], a[184:187], v[68:71]\n\t"
    "buffer_load_dwordx4 v246, s[48:51], s66 offen lds\n\t"
    "v_mfma_f32_16x16x32_bf16 v[68:71], v[132:135], a[188:191], v[68:71]\n\t"
    "ds_read_b64_tr_b16 v[146:147], v228 offset:17152\n\t"
    "v_mfma_f32_16x16x32_bf16 v[72:75], v[128:131], a[16:19], v[72:75]\n\t"
    "ds_read_b64_tr_b16 v[148:149], v228 offset:20736\n\t"
    "s_mov_b32 s66, 2686976\n\t"
    "v_mfma_f32_16x16x32_bf16 v[72:75], v[132:135], a[20:23], v[72:75]\n\t"
    "ds_read_b64_tr_b16 v[150:151], v228 offset:21248\n\t"
    "s_add_u32 m0, s64, 53248\n\t"
    "v_mfma_f32_16x16x32_bf16 v[76:79], v[128:131], a[24:27], v[76:79]\n\t"
    "buffer_load_dwordx4 v246, s[48:51], s66 offen lds\n\t"
    "v_mfma_f32_16x16x32_bf16 v[76:79], v[132:135], a[28:31], v[76:79]\n\t"
    "s_waitcnt lgkmcnt(4)\n\t"
    "v_mfma_f32_16x16x32_bf16 v[80:83], v[136:139], a[176:179], v[80:83]\n\t"
    "ds_read_b64_tr_b16 v[152:153], v229 offset:16640\n\t"
    "s_mov_b32 s66, 2752512\n\t"
    "v_mfma_f32_16x16x32_bf16 v[80:83], v[140:143], a[180:183], v[80:83]\n\t"
    "s_add_u32 m0, s64, 57344\n\t"
    "v_mfma_f32_16x16x32_bf16 v[84:87], v[136:139], a[184:187], v[84:87]\n\t"
    "buffer_load_dwordx4 v232, s[48:51], s66 offen lds\n\t"
    "v_mfma_f32_16x16x32_bf16 v[84:87], v[140:143], a[188:191], v[84:87]\n\t"
    "ds_read_b64_tr_b16 v[154:155], v229 offset:17152\n\t"
    "v_mfma_f32_16x16x32_bf16 v[88:91], v[136:139], a[16:19], v[88:91]\n\t"
    "ds_read_b64_tr_b16 v[156:157], v229 offset:20736\n\t"
    "s_mov_b32 s66, 2818048\n\t"
    "v_mfma_f32_16x16x32_bf16 v[88:91], v[140:143], a[20:23], v[88:91]\n\t"
    "ds_read_b64_tr_b16 v[158:159], v229 offset:21248\n\t"
    "s_add_u32 m0, s64, 61440\n\t"
    "v_mfma_f32_16x16x32_bf16 v[92:95], v[136:139], a[24:27], v[92:95]\n\t"
    "buffer_load_dwordx4 v232, s[48:51], s66 offen lds\n\t"
    "v_mfma_f32_16x16x32_bf16 v[92:95], v[140:143], a[28:31], v[92:95]\n\t"
    "s_waitcnt vmcnt(20)\n\t"
    "s_barrier\n\t"
    "s_waitcnt lgkmcnt(4)\n\t"
    "v_mfma_f32_16x16x32_bf16 v[96:99], v[144:147], a[176:179], v[96:99]\n\t"
    "ds_read_b64_tr_b16 v[128:129], v224 offset:0\n\t"
    "s_mov_b32 s66, 2883584\n\t"
    "v_mfma_f32_16x16x32_bf16 v[96:99], v[148:151], a[180:183], v[96:99]\n\t"
    "s_add_u32 m0, s64, 98304\n\t"
    "v_mfma_f32_16x16x32_bf16 v[100:103], v[144:147], a[184:187], v[100:103]\n\t"
    "buffer_load_dwordx4 v246, s[48:51], s66 offen lds\n\t"
    "v_mfma_f32_16x16x32_bf16 v[100:103], v[148:151], a[188:191], v[100:103]\n\t"
    "ds_read_b64_tr_b16 v[130:131], v224 offset:512\n\t"
    "v_mfma_f32_16x16x32_bf16 v[104:107], v[144:147], a[16:19], v[104:107]\n\t"
    "ds_read_b64_tr_b16 v[132:133], v224 offset:4096\n\t"
    "s_mov_b32 s66, 2949120\n\t"
    "v_mfma_f32_16x16x32_bf16 v[104:107], v[148:151], a[20:23], v[104:107]\n\t"
    "ds_read_b64_tr_b16 v[134:135], v224 offset:4608\n\t"
    "s_add_u32 m0, s64, 102400\n\t"
    "v_mfma_f32_16x16x32_bf16 v[108:111], v[144:147], a[24:27], v[108:111]\n\t"
    "buffer_load_dwordx4 v246, s[48:51], s66 offen lds\n\t"
    "v_mfma_f32_16x16x32_bf16 v[108:111], v[148:151], a[28:31], v[108:111]\n\t"
    "s_waitcnt lgkmcnt(4)\n\t"
    "v_mfma_f32_16x16x32_bf16 v[112:115], v[152:155], a[176:179], v[112:115]\n\t"
    "ds_read_b64_tr_b16 v[136:137], v225 offset:0\n\t"
    "s_mov_b32 s66, 3014656\n\t"
    "v_mfma_f32_16x16x32_bf16 v[112:115], v[156:159], a[180:183], v[112:115]\n\t"
    "s_add_u32 m0, s64, 106496\n\t"
    "v_mfma_f32_16x16x32_bf16 v[116:119], v[152:155], a[184:187], v[116:119]\n\t"
    "buffer_load_dwordx4 v232, s[48:51], s66 offen lds\n\t"
    "v_mfma_f32_16x16x32_bf16 v[116:119], v[156:159], a[188:191], v[116:119]\n\t"
    "ds_read_b64_tr_b16 v[138:139], v225 offset:512\n\t"
    "v_mfma_f32_16x16x32_bf16 v[120:123], v[152:155], a[16:19], v[120:123]\n\t"
    "ds_read_b64_tr_b16 v[140:141], v225 offset:4096\n\t"
    "s_mov_b32 s66, 3080192\n\t"
    "v_mfma_f32_16x16x32_bf16 v[120:123], v[156:159], a[20:23], v[120:123]\n\t"
    "ds_read_b64_tr_b16 v[142:143], v225 offset:4608\n\t"
    "s_add_u32 m0, s64, 110592\n\t"
    "v_mfma_f32_16x16x32_bf16 v[124:127], v[152:155], a[24:27], v[124:127]\n\t"
    "buffer_load_dwordx4 v232, s[48:51], s66 offen lds\n\t"
    "v_mfma_f32_16x16x32_bf16 v[124:127], v[156:159], a[28:31], v[124:127]\n\t"
    "s_waitcnt vmcnt(63) lgkmcnt(4)\n\t"
    "v_mfma_f32_16x16x32_bf16 v[0:3], v[128:131], a[160:163], v[0:3]\n\t"
    "ds_read_b64_tr_b16 v[144:145], v224 offset:256\n\t"
    "v_mfma_f32_16x16x32_bf16 v[0:3], v[132:135], a[164:167], v[0:3]\n\t"
    "s_mov_b32 s67, 40960\n\t"
    "v_mfma_f32_16x16x32_bf16 v[4:7], v[128:131], a[168:171], v[4:7]\n\t"
    "buffer_load_dwordx4 a[96:99], v252, s[52:55], s67 offen offset:0 sc1\n\t"
    "v_mfma_f32_16x16x32_bf16 v[4:7], v[132:135], a[172:175], v[4:7]\n\t"
    "ds_read_b64_tr_b16 v[146:147], v224 offset:768\n\t"
    "s_waitcnt vmcnt(37)\n\t"
    "v_mfma_f32_16x16x32_bf16 v[8:11], v[128:131], a[0:3], v[8:11]\n\t"
    "ds_read_b64_tr_b16 v[148:149], v224 offset:4352\n\t"
    "v_mfma_f32_16x16x32_bf16 v[8:11], v[132:135], a[4:7], v[8:11]\n\t"
    "ds_read_b64_tr_b16 v[150:151], v224 offset:4864\n\t"
    "v_mfma_f32_16x16x32_bf16 v[12:15], v[128:131], a[8:11], v[12:15]\n\t"
    "buffer_load_dwordx4 a[100:103], v252, s[52:55], s67 offen offset:1024 sc1\n\t"
    "v_mfma_f32_16x16x32_bf16 v[12:15], v[132:135], a[12:15], v[12:15]\n\t"
    "s_waitcnt lgkmcnt(4)\n\t"
    "v_mfma_f32_16x16x32_bf16 v[16:19], v[136:139], a[160:163], v[16:19]\n\t"
    "ds_read_b64_tr_b16 v[152:153], v225 offset:256\n\t"
    "v_mfma_f32_16x16x32_bf16 v[16:19], v[140:143], a[164:167], v[16:19]\n\t"
    "v_mfma_f32_16x16x32_bf16 v[20:23], v[136:139], a[168:171], v[20:23]\n\t"
    "buffer_load_dwordx4 a[104:107], v230, s[52:55], s67 offen offset:0 sc1\n\t"
    "v_mfma_f32_16x16x32_bf16 v[20:23], v[140:143], a[172:175], v[20:23]\n\t"
    "ds_read_b64_tr_b16 v[154:155], v225 offset:768\n\t"
    "v_mfma_f32_16x16x32_bf16 v[24:27], v[136:139], a[0:3], v[24:27]\n\t"
    "ds_read_b64_tr_b16 v[156:157], v225 offset:4352\n\t"
    "v_mfma_f32_16x16x32_bf16 v[24:27], v[140:143], a[4:7], v[24:27]\n\t"
    "ds_read_b64_tr_b16 v[158:159], v225 offset:4864\n\t"
    "v_mfma_f32_16x16x32_bf16 v[28:31], v[136:139], a[8:11], v[28:31]\n\t"
    "buffer_load_dwordx4 a[108:111], v230, s[52:55], s67 offen offset:1024 sc1\n\t"
    "v_mfma_f32_16x16x32_bf16 v[28:31], v[140:143], a[12:15], v[28:31]\n\t"
    "s_waitcnt lgkmcnt(4)\n\t"
    "v_mfma_f32_16x16x32_bf16 v[32:35], v[144:147], a[160:163], v[32:35]\n\t"
    "ds_read_b64_tr_b16 v[128:129], v224 offset:16384\n\t"
    "v_mfma_f32_16x16x32_bf16 v[32:35], v[148:151], a[164:167], v[32:35]\n\t"
    "s_mov_b32 s67, 499712\n\t"
    "v_mfma_f32_16x16x32_bf16 v[36:39], v[144:147], a[168:171], v[36:39]\n\t"
    "buffer_load_dwordx4 v[208:211], v252, s[52:55], s67 offen offset:0 sc1\n\t"
    "v_mfma_f32_16x16x32_bf16 v[36:39], v[148:151], a[172:175], v[36:39]\n\t"
    "ds_read_b64_tr_b16 v[130:131], v224 offset:16896\n\t"
    "v_mfma_f32_16x16x32_bf16 v[40:43], v[144:147], a[0:3], v[40:43]\n\t"
    "ds_read_b64_tr_b16 v[132:133], v224 offset:20480\n\t"
    "v_mfma_f32_16x16x32_bf16 v[40:43], v[148:151], a[4:7], v[40:43]\n\t"
    "ds_read_b64_tr_b16 v[134:135], v224 offset:20992\n\t"
    "v_mfma_f32_16x16x32_bf16 v[44:47], v[144:147], a[8:11], v[44:47]\n\t"
    "buffer_load_dwordx4 v[212:215], v252, s[52:55], s67 offen offset:1024 sc1\n\t"
    "v_mfma_f32_16x16x32_bf16 v[44:47], v[148:151], a[12:15], v[44:47]\n\t"
    "s_waitcnt lgkmcnt(4)\n\t"
    "v_mfma_f32_16x16x32_bf16 v[48:51], v[152:155], a[160:163], v[48:51]\n\t"
    "ds_read_b64_tr_b16 v[136:137], v225 offset:16384\n\t"
    "v_mfma_f32_16x16x32_bf16 v[48:51], v[156:159], a[164:167], v[48:51]\n\t"
    "v_mfma_f32_16x16x32_bf16 v[52:55], v[152:155], a[168:171], v[52:55]\n\t"
    "buffer_load_dwordx4 v[216:219], v230, s[52:55], s67 offen offset:0 sc1\n\t"
    "v_mfma_f32_16x16x32_bf16 v[52:55], v[156:159], a[172:175], v[52:55]\n\t"
    "ds_read_b64_tr_b16 v[138:139], v225 offset:16896\n\t"
    "v_mfma_f32_16x16x32_bf16 v[56:59], v[152:155], a[0:3], v[56:59]\n\t"
    "ds_read_b64_tr_b16 v[140:141], v225 offset:20480\n\t"
    "v_mfma_f32_16x16x32_bf16 v[56:59], v[156:159], a[4:7], v[56:59]\n\t"
    "ds_read_b64_tr_b16 v[142:143], v225 offset:20992\n\t"
    "v_mfma_f32_16x16x32_bf16 v[60:63], v[152:155], a[8:11], v[60:63]\n\t"
    "buffer_load_dwordx4 v[220:223], v230, s[52:55], s67 offen offset:1024 sc1\n\t"
    "v_mfma_f32_16x16x32_bf16 v[60:63], v[156:159], a[12:15], v[60:63]\n\t"
    "s_waitcnt vmcnt(63) lgkmcnt(4)\n\t"
    "v_mfma_f32_16x16x32_bf16 v[0:3], v[128:131], a[144:147], v[0:3]\n\t"
    "ds_read_b64_tr_b16 v[144:145], v224 offset:16640\n\t"
    "v_mfma_f32_16x16x32_bf16 v[0:3], v[132:135], a[148:151], v[0:3]\n\t"
    "s_mov_b32 s67, 45056\n\t"
    "v_mfma_f32_16x16x32_bf16 v[4:7], v[128:131], a[152:155], v[4:7]\n\t"
    "buffer_load_dwordx4 a[80:83], v252, s[52:55], s67 offen offset:0 sc1\n\t"
    "v_mfma_f32_16x16x32_bf16 v[4:7], v[132:135], a[156:159], v[4:7]\n\t"
    "ds_read_b64_tr_b16 v[146:147], v224 offset:17152\n\t"
    "s_waitcnt vmcnt(41)\n\t"
    "v_mfma_f32_16x16x32_bf16 v[8:11], v[128:131], v[160:163], v[8:11]\n\t"
    "ds_read_b64_tr_b16 v[148:149], v224 offset:20736\n\t"
    "v_mfma_f32_16x16x32_bf16 v[8:11], v[132:135], v[164:167], v[8:11]\n\t"
    "ds_read_b64_tr_b16 v[150:151], v224 offset:21248\n\t"
    "v_mfma_f32_16x16x32_bf16 v[12:15], v[128:131], v[168:171], v[12:15]\n\t"
    "buffer_load_dwordx4 a[84:87], v252, s[52:55], s67 offen offset:1024 sc1\n\t"
    "v_mfma_f32_16x16x32_bf16 v[12:15], v[132:135], v[172:175], v[12:15]\n\t"
    "s_waitcnt lgkmcnt(4)\n\t"
    "v_mfma_f32_16x16x32_bf16 v[16:19], v[136:139], a[144:147], v[16:19]\n\t"
    "ds_read_b64_tr_b16 v[152:153], v225 offset:16640\n\t"
    "v_mfma_f32_16x16x32_bf16 v[16:19], v[140:143], a[148:151], v[16:19]\n\t"
    "v_mfma_f32_16x16x32_bf16 v[20:23], v[136:139], a[152:155], v[20:23]\n\t"
    "buffer_load_dwordx4 a[88:91], v230, s[52:55], s67 offen offset:0 sc1\n\t"
    "v_mfma_f32_16x16x32_bf16 v[20:23], v[140:143], a[156:159], v[20:23]\n\t"
    "ds_read_b64_tr_b16 v[154:155], v225 offset:17152\n\t"
    "v_mfma_f32_16x16x32_bf16 v[24:27], v[136:139], v[160:163], v[24:27]\n\t"
    "ds_read_b64_tr_b16 v[156:157], v225 offset:20736\n\t"
    "v_mfma_f32_16x16x32_bf16 v[24:27], v[140:143], v[164:167], v[24:27]\n\t"
    "ds_read_b64_tr_b16 v[158:159], v225 offset:21248\n\t"
    "v_mfma_f32_16x16x32_bf16 v[28:31], v[136:139], v[168:171], v[28:31]\n\t"
    "buffer_load_dwordx4 a[92:95], v230, s[52:55], s67 offen offset:1024 sc1\n\t"
    "v_mfma_f32_16x16x32_bf16 v[28:31], v[140:143], v[172:175], v[28:31]\n\t"
    "s_waitcnt vmcnt(28)\n\t"
    "s_barrier\n\t"
    "s_waitcnt lgkmcnt(4)\n\t"
    "v_mfma_f32_16x16x32_bf16 v[32:35], v[144:147], a[144:147], v[32:35]\n\t"
    "ds_read_b64_tr_b16 v[128:129], v226 offset:0\n\t"
    "v_mfma_f32_16x16x32_bf16 v[32:35], v[148:151], a[148:151], v[32:35]\n\t"
    "s_mov_b32 s67, 503808\n\t"
    "v_mfma_f32_16x16x32_bf16 v[36:39], v[144:147], a[152:155], v[36:39]\n\t"
    "buffer_load_dwordx4 a[240:243], v252, s[52:55], s67 offen offset:0 sc1\n\t"
    "v_mfma_f32_16x16x32_bf16 v[36:39], v[148:151], a[156:159], v[36:39]\n\t"
    "ds_read_b64_tr_b16 v[130:131], v226 offset:512\n\t"
    "v_mfma_f32_16x16x32_bf16 v[40:43], v[144:147], v[160:163], v[40:43]\n\t"
    "ds_read_b64_tr_b16 v[132:133], v226 offset:4096\n\t"
    "v_mfma_f32_16x16x32_bf16 v[40:43], v[148:151], v[164:167], v[40:43]\n\t"
    "ds_read_b64_tr_b16 v[134:135], v226 offset:4608\n\t"
    "v_mfma_f32_16x16x32_bf16 v[44:47], v[144:147], v[168:171], v[44:47]\n\t"
    "buffer_load_dwordx4 a[244:247], v252, s[52:55], s67 offen offset:1024 sc1\n\t"
    "v_mfma_f32_16x16x32_bf16 v[44:47], v[148:151], v[172:175], v[44:47]\n\t"
    "s_waitcnt lgkmcnt(4)\n\t"
    "v_mfma_f32_16x16x32_bf16 v[48:51], v[152:155], a[144:147], v[48:51]\n\t"
    "ds_read_b64_tr_b16 v[136:137], v227 offset:0\n\t"
    "v_mfma_f32_16x16x32_bf16 v[48:51], v[156:159], a[148:151], v[48:51]\n\t"
    "v_mfma_f32_16x16x32_bf16 v[52:55], v[152:155], a[152:155], v[52:55]\n\t"
    "buffer_load_dwordx4 a[248:251], v230, s[52:55], s67 offen offset:0 sc1\n\t"
    "v_mfma_f32_16x16x32_bf16 v[52:55], v[156:159], a[156:159], v[52:55]\n\t"
    "ds_read_b64_tr_b16 v[138:139], v227 offset:512\n\t"
    "v_mfma_f32_16x16x32_bf16 v[56:59], v[152:155], v[160:163], v[56:59]\n\t"
    "ds_read_b64_tr_b16 v[140:141], v227 offset:4096\n\t"
    "v_mfma_f32_16x16x32_bf16 v[56:59], v[156:159], v[164:167], v[56:59]\n\t"
    "ds_read_b64_tr_b16 v[142:143], v227 offset:4608\n\t"
    "v_mfma_f32_16x16x32_bf16 v[60:63], v[152:155], v[168:171], v[60:63]\n\t"
    "buffer_load_dwordx4 a[252:255], v230, s[52:55], s67 offen offset:1024 sc1\n\t"
    "v_mfma_f32_16x16x32_bf16 v[60:63], v[156:159], v[172:175], v[60:63]\n\t"
    "s_waitcnt vmcnt(63) lgkmcnt(4)\n\t"
    "v_mfma_f32_16x16x32_bf16 v[0:3], v[128:131], a[128:131], v[0:3]\n\t"
    "ds_read_b64_tr_b16 v[144:145], v226 offset:256\n\t"
    "s_mov_b32 s66, 3145728\n\t"
    "v_mfma_f32_16x16x32_bf16 v[0:3], v[132:135], a[132:135], v[0:3]\n\t"
    "s_add_u32 m0, s64, 81920\n\t"
    "v_mfma_f32_16x16x32_bf16 v[4:7], v[128:131], a[136:139], v[4:7]\n\t"
    "buffer_load_dwordx4 v246, s[48:51], s66 offen lds\n\t"
    "v_mfma_f32_16x16x32_bf16 v[4:7], v[132:135], a[140:143], v[4:7]\n\t"
    "ds_read_b64_tr_b16 v[146:147], v226 offset:768\n\t"
    "s_waitcnt vmcnt(29)\n\t"
    "v_mfma_f32_16x16x32_bf16 v[8:11], v[128:131], v[176:179], v[8:11]\n\t"
    "ds_read_b64_tr_b16 v[148:149], v226 offset:4352\n\t"
    "s_mov_b32 s66, 3211264\n\t"
    "v_mfma_f32_16x16x32_bf16 v[8:11], v[132:135], v[180:183], v[8:11]\n\t"
    "ds_read_b64_tr_b16 v[150:151], v226 offset:4864\n\t"
    "s_add_u32 m0, s64, 86016\n\t"
    "v_mfma_f32_16x16x32_bf16 v[12:15], v[128:131], v[184:187], v[12:15]\n\t"
    "buffer_load_dwordx4 v246, s[48:51], s66 offen lds\n\t"
    "v_mfma_f32_16x16x32_bf16 v[12:15], v[132:135], v[188:191], v[12:15]\n\t"
    "s_waitcnt lgkmcnt(4)\n\t"
    "v_mfma_f32_16x16x32_bf16 v[16:19], v[136:139], a[128:131], v[16:19]\n\t"
    "ds_read_b64_tr_b16 v[152:153], v227 offset:256\n\t"
    "s_mov_b32 s66, 3276800\n\t"
    "v_mfma_f32_16x16x32_bf16 v[16:19], v[140:143], a[132:135], v[16:19]\n\t"
    "s_add_u32 m0, s64, 90112\n\t"
    "v_mfma_f32_16x16x32_bf16 v[20:23], v[136:139], a[136:139], v[20:23]\n\t"
    "buffer_load_dwordx4 v232, s[48:51], s66 offen lds\n\t"
    "v_mfma_f32_16x16x32_bf16 v[20:23], v[140:143], a[140:143], v[20:23]\n\t"
    "ds_read_b64_tr_b16 v[154:155], v227 offset:768\n\t"
    "v_mfma_f32_16x16x32_bf16 v[24:27], v[136:139], v[176:179], v[24:27]\n\t"
    "ds_read_b64_tr_b16 v[156:157], v227 offset:4352\n\t"
    "s_mov_b32 s66, 3342336\n\t"
    "v_mfma_f32_16x16x32_bf16 v[24:27], v[140:143], v[180:183], v[24:27]\n\t"
    "ds_read_b64_tr_b16 v[158:159], v227 offset:4864\n\t"
    "s_add_u32 m0, s64, 94208\n\t"
    "v_mfma_f32_16x16x32_bf16 v[28:31], v[136:139], v[184:187], v[28:31]\n\t"
    "buffer_load_dwordx4 v232, s[48:51], s66 offen lds\n\t"
    "v_mfma_f32_16x16x32_bf16 v[28:31], v[140:143], v[188:191], v[28:31]\n\t"
    "s_waitcnt lgkmcnt(4)\n\t"
    "v_mfma_f32_16x16x32_bf16 v[32:35], v[144:147], a[128:131], v[32:35]\n\t"
    "ds_read_b64_tr_b16 v[128:129], v224 offset:32768\n\t"
    "s_mov_b32 s66, 3407872\n\t"
    "v_mfma_f32_16x16x32_bf16 v[32:35], v[148:151], a[132:135], v[32:35]\n\t"
    "s_add_u32 m0, s64, 131072\n\t"
    "v_mfma_f32_16x16x32_bf16 v[36:39], v[144:147], a[136:139], v[36:39]\n\t"
    "buffer_load_dwordx4 v246, s[48:51], s66 offen lds\n\t"
    "v_mfma_f32_16x16x32_bf16 v[36:39], v[148:151], a[140:143], v[36:39]\n\t"
    "ds_read_b64_tr_b16 v[130:131], v224 offset:33280\n\t"
    "v_mfma_f32_16x16x32_bf16 v[40:43], v[144:147], v[176:179], v[40:43]\n\t"
    "ds_read_b64_tr_b16 v[132:133], v224 offset:36864\n\t"
    "s_mov_b32 s66, 3473408\n\t"
    "v_mfma_f32_16x16x32_bf16 v[40:43], v[148:151], v[180:183], v[40:43]\n\t"
    "ds_read_b64_tr_b16 v[134:135], v224 offset:37376\n\t"
    "s_add_u32 m0, s64, 135168\n\t"
    "v_mfma_f32_16x16x32_bf16 v[44:47], v[144:147], v[184:187], v[44:47]\n\t"
    "buffer_load_dwordx4 v246, s[48:51], s66 offen lds\n\t"
    "v_mfma_f32_16x16x32_bf16 v[44:47], v[148:151], v[188:191], v[44:47]\n\t"
    "s_waitcnt lgkmcnt(4)\n\t"
    "v_mfma_f32_16x16x32_bf16 v[48:51], v[152:155], a[128:131], v[48:51]\n\t"
    "ds_read_b64_tr_b16 v[136:137], v225 offset:32768\n\t"
    "s_mov_b32 s66, 3538944\n\t"
    "v_mfma_f32_16x16x32_bf16 v[48:51], v[156:159], a[132:135], v[48:51]\n\t"
    "s_add_u32 m0, s64, 139264\n\t"
    "v_mfma_f32_16x16x32_bf16 v[52:55], v[152:155], a[136:139], v[52:55]\n\t"
    "buffer_load_dwordx4 v232, s[48:51], s66 offen lds\n\t"
    "v_mfma_f32_16x16x32_bf16 v[52:55], v[156:159], a[140:143], v[52:55]\n\t"
    "ds_read_b64_tr_b16 v[138:139], v225 offset:33280\n\t"
    "v_mfma_f32_16x16x32_bf16 v[56:59], v[152:155], v[176:179], v[56:59]\n\t"
    "ds_read_b64_tr_b16 v[140:141], v225 offset:36864\n\t"
    "s_mov_b32 s66, 3604480\n\t"
    "v_mfma_f32_16x16x32_bf16 v[56:59], v[156:159], v[180:183], v[56:59]\n\t"
    "ds_read_b64_tr_b16 v[142:143], v225 offset:37376\n\t"
    "s_add_u32 m0, s64, 143360\n\t"
    "v_mfma_f32_16x16x32_bf16 v[60:63], v[152:155], v[184:187], v[60:63]\n\t"
    "buffer_load_dwordx4 v232, s[48:51], s66 offen lds\n\t"
    "v_mfma_f32_16x16x32_bf16 v[60:63], v[156:159], v[188:191], v[60:63]\n\t"
    "s_waitcnt vmcnt(63) lgkmcnt(4)\n\t"
    "v_mfma_f32_16x16x32_bf16 v[0:3], v[128:131], a[112:115], v[0:3]\n\t"
    "ds_read_b64_tr_b16 v[144:145], v224 offset:33024\n\t"
    "v_mfma_f32_16x16x32_bf16 v[0:3], v[132:135], a[116:119], v[0:3]\n\t"
    "s_mov_b32 s67, 49152\n\t"
    "v_mfma_f32_16x16x32_bf16 v[4:7], v[128:131], a[120:123], v[4:7]\n\t"
    "buffer_load_dwordx4 a[64:67], v252, s[52:55], s67 offen offset:0 sc1\n\t"
    "v_mfma_f32_16x16x32_bf16 v[4:7], v[132:135], a[124:127], v[4:7]\n\t"
    "ds_read_b64_tr_b16 v[146:147], v224 offset:33536\n\t"
    "s_waitcnt vmcnt(33)\n\t"
    "v_mfma_f32_16x16x32_bf16 v[8:11], v[128:131], v[192:195], v[8:11]\n\t"
    "ds_read_b64_tr_b16 v[148:149], v224 offset:37120\n\t"
    "v_mfma_f32_16x16x32_bf16 v[8:11], v[132:135], v[196:199], v[8:11]\n\t"
    "ds_read_b64_tr_b16 v[150:151], v224 offset:37632\n\t"
    "v_mfma_f32_16x16x32_bf16 v[12:15], v[128:131], v[200:203], v[12:15]\n\t"
    "buffer_load_dwordx4 a[68:71], v252, s[52:55], s67 offen offset:1024 sc1\n\t"
    "v_mfma_f32_16x16x32_bf16 v[12:15], v[132:135], v[204:207], v[12:15]\n\t"
    "s_waitcnt lgkmcnt(4)\n\t"
    "v_mfma_f32_16x16x32_bf16 v[16:19], v[136:139], a[112:115], v[16:19]\n\t"
    "ds_read_b64_tr_b16 v[152:153], v225 offset:33024\n\t"
    "v_mfma_f32_16x16x32_bf16 v[16:19], v[140:143], a[116:119], v[16:19]\n\t"
    "v_mfma_f32_16x16x32_bf16 v[20:23], v[136:139], a[120:123], v[20:23]\n\t"
    "buffer_load_dwordx4 a[72:75], v230, s[52:55], s67 offen offset:0 sc1\n\t"
    "v_mfma_f32_16x16x32_bf16 v[20:23], v[140:143], a[124:127], v[20:23]\n\t"
    "ds_read_b64_tr_b16 v[154:155], v225 offset:33536\n\t"
    "v_mfma_f32_16x16x32_bf16 v[24:27], v[136:139], v[192:195], v[24:27]\n\t"
    "ds_read_b64_tr_b16 v[156:157], v225 offset:37120\n\t"
    "v_mfma_f32_16x16x32_bf16 v[24:27], v[140:143], v[196:199], v[24:27]\n\t"
    "ds_read_b64_tr_b16 v[158:159], v225 offset:37632\n\t"
    "v_mfma_f32_16x16x32_bf16 v[28:31], v[136:139], v[200:203], v[28:31]\n\t"
    "buffer_load_dwordx4 a[76:79], v230, s[52:55], s67 offen offset:1024 sc1\n\t"
    "v_mfma_f32_16x16x32_bf16 v[28:31], v[140:143], v[204:207], v[28:31]\n\t"
    "s_waitcnt vmcnt(28)\n\t"
    "s_barrier\n\t"
    "s_waitcnt lgkmcnt(4)\n\t"
    "v_mfma_f32_16x16x32_bf16 v[32:35], v[144:147], a[112:115], v[32:35]\n\t"
    "ds_read_b64_tr_b16 v[128:129], v224 offset:49152\n\t"
    "v_mfma_f32_16x16x32_bf16 v[32:35], v[148:151], a[116:119], v[32:35]\n\t"
    "s_mov_b32 s67, 507904\n\t"
    "v_mfma_f32_16x16x32_bf16 v[36:39], v[144:147], a[120:123], v[36:39]\n\t"
    "buffer_load_dwordx4 a[224:227], v252, s[52:55], s67 offen offset:0 sc1\n\t"
    "v_mfma_f32_16x16x32_bf16 v[36:39], v[148:151], a[124:127], v[36:39]\n\t"
    "ds_read_b64_tr_b16 v[130:131], v224 offset:49664\n\t"
    "v_mfma_f32_16x16x32_bf16 v[40:43], v[144:147], v[192:195], v[40:43]\n\t"
    "ds_read_b64_tr_b16 v[132:133], v224 offset:53248\n\t"
    "v_mfma_f32_16x16x32_bf16 v[40:43], v[148:151], v[196:199], v[40:43]\n\t"
    "ds_read_b64_tr_b16 v[134:135], v224 offset:53760\n\t"
    "v_mfma_f32_16x16x32_bf16 v[44:47], v[144:147], v[200:203], v[44:47]\n\t"
    "buffer_load_dwordx4 a[228:231], v252, s[52:55], s67 offen offset:1024 sc1\n\t"
    "v_mfma_f32_16x16x32_bf16 v[44:47], v[148:151], v[204:207], v[44:47]\n\t"
    "s_waitcnt lgkmcnt(4)\n\t"
    "v_mfma_f32_16x16x32_bf16 v[48:51], v[152:155], a[112:115], v[48:51]\n\t"
    "ds_read_b64_tr_b16 v[136:137], v225 offset:49152\n\t"
    "v_mfma_f32_16x16x32_bf16 v[48:51], v[156:159], a[116:119], v[48:51]\n\t"
    "v_mfma_f32_16x16x32_bf16 v[52:55], v[152:155], a[120:123], v[52:55]\n\t"
    "buffer_load_dwordx4 a[232:235], v230, s[52:55], s67 offen offset:0 sc1\n\t"
    "v_mfma_f32_16x16x32_bf16 v[52:55], v[156:159], a[124:127], v[52:55]\n\t"
    "ds_read_b64_tr_b16 v[138:139], v225 offset:49664\n\t"
    "v_mfma_f32_16x16x32_bf16 v[56:59], v[152:155], v[192:195], v[56:59]\n\t"
    "ds_read_b64_tr_b16 v[140:141], v225 offset:53248\n\t"
    "v_mfma_f32_16x16x32_bf16 v[56:59], v[156:159], v[196:199], v[56:59]\n\t"
    "ds_read_b64_tr_b16 v[142:143], v225 offset:53760\n\t"
    "v_mfma_f32_16x16x32_bf16 v[60:63], v[152:155], v[200:203], v[60:63]\n\t"
    "buffer_load_dwordx4 a[236:239], v230, s[52:55], s67 offen offset:1024 sc1\n\t"
    "v_mfma_f32_16x16x32_bf16 v[60:63], v[156:159], v[204:207], v[60:63]\n\t"
    "s_waitcnt vmcnt(28) lgkmcnt(4)\n\t"
    "v_mfma_f32_16x16x32_bf16 v[0:3], v[128:131], a[96:99], v[0:3]\n\t"
    "ds_read_b64_tr_b16 v[144:145], v224 offset:49408\n\t"
    "v_mfma_f32_16x16x32_bf16 v[0:3], v[132:135], a[100:103], v[0:3]\n\t"
    "s_mov_b32 s67, 53248\n\t"
    "v_mfma_f32_16x16x32_bf16 v[4:7], v[128:131], a[104:107], v[4:7]\n\t"
    "buffer_load_dwordx4 a[48:51], v252, s[52:55], s67 offen offset:0 sc1\n\t"
    "v_mfma_f32_16x16x32_bf16 v[4:7], v[132:135], a[108:111], v[4:7]\n\t"
    "ds_read_b64_tr_b16 v[146:147], v224 offset:49920\n\t"
    "s_waitcnt vmcnt(25)\n\t"
    "v_mfma_f32_16x16x32_bf16 v[8:11], v[128:131], v[208:211], v[8:11]\n\t"
    "ds_read_b64_tr_b16 v[148:149], v224 offset:53504\n\t"
    "v_mfma_f32_16x16x32_bf16 v[8:11], v[132:135], v[212:215], v[8:11]\n\t"
    "ds_read_b64_tr_b16 v[150:151], v224 offset:54016\n\t"
    "v_mfma_f32_16x16x32_bf16 v[12:15], v[128:131], v[216:219], v[12:15]\n\t"
    "buffer_load_dwordx4 a[52:55], v252, s[52:55], s67 offen offset:1024 sc1\n\t"
    "v_mfma_f32_16x16x32_bf16 v[12:15], v[132:135], v[220:223], v[12:15]\n\t"
    "s_waitcnt lgkmcnt(4)\n\t"
    "v_mfma_f32_16x16x32_bf16 v[16:19], v[136:139], a[96:99], v[16:19]\n\t"
    "ds_read_b64_tr_b16 v[152:153], v225 offset:49408\n\t"
    "v_mfma_f32_16x16x32_bf16 v[16:19], v[140:143], a[100:103], v[16:19]\n\t"
    "v_mfma_f32_16x16x32_bf16 v[20:23], v[136:139], a[104:107], v[20:23]\n\t"
    "buffer_load_dwordx4 a[56:59], v230, s[52:55], s67 offen offset:0 sc1\n\t"
    "v_mfma_f32_16x16x32_bf16 v[20:23], v[140:143], a[108:111], v[20:23]\n\t"
    "ds_read_b64_tr_b16 v[154:155], v225 offset:49920\n\t"
    "v_mfma_f32_16x16x32_bf16 v[24:27], v[136:139], v[208:211], v[24:27]\n\t"
    "ds_read_b64_tr_b16 v[156:157], v225 offset:53504\n\t"
    "v_mfma_f32_16x16x32_bf16 v[24:27], v[140:143], v[212:215], v[24:27]\n\t"
    "ds_read_b64_tr_b16 v[158:159], v225 offset:54016\n\t"
    "v_mfma_f32_16x16x32_bf16 v[28:31], v[136:139], v[216:219], v[28:31]\n\t"
    "buffer_load_dwordx4 a[60:63], v230, s[52:55], s67 offen offset:1024 sc1\n\t"
    "v_mfma_f32_16x16x32_bf16 v[28:31], v[140:143], v[220:223], v[28:31]\n\t"
    "s_waitcnt lgkmcnt(4)\n\t"
    "v_mfma_f32_16x16x32_bf16 v[32:35], v[144:147], a[96:99], v[32:35]\n\t"
    "ds_read_b64_tr_b16 v[128:129], v226 offset:32768\n\t"
    "v_mfma_f32_16x16x32_bf16 v[32:35], v[148:151], a[100:103], v[32:35]\n\t"
    "s_mov_b32 s67, 512000\n\t"
    "v_mfma_f32_16x16x32_bf16 v[36:39], v[144:147], a[104:107], v[36:39]\n\t"
    "buffer_load_dwordx4 a[208:211], v252, s[52:55], s67 offen offset:0 sc1\n\t"
    "v_mfma_f32_16x16x32_bf16 v[36:39], v[148:151], a[108:111], v[36:39]\n\t"
    "ds_read_b64_tr_b16 v[130:131], v226 offset:33280\n\t"
    "v_mfma_f32_16x16x32_bf16 v[40:43], v[144:147], v[208:211], v[40:43]\n\t"
    "ds_read_b64_tr_b16 v[132:133], v226 offset:36864\n\t"
    "v_mfma_f32_16x16x32_bf16 v[40:43], v[148:151], v[212:215], v[40:43]\n\t"
    "ds_read_b64_tr_b16 v[134:135], v226 offset:37376\n\t"
    "v_mfma_f32_16x16x32_bf16 v[44:47], v[144:147], v[216:219], v[44:47]\n\t"
    "buffer_load_dwordx4 a[212:215], v252, s[52:55], s67 offen offset:1024 sc1\n\t"
    "v_mfma_f32_16x16x32_bf16 v[44:47], v[148:151], v[220:223], v[44:47]\n\t"
    "s_waitcnt lgkmcnt(4)\n\t"
    "v_mfma_f32_16x16x32_bf16 v[48:51], v[152:155], a[96:99], v[48:51]\n\t"
    "ds_read_b64_tr_b16 v[136:137], v227 offset:32768\n\t"
    "v_mfma_f32_16x16x32_bf16 v[48:51], v[156:159], a[100:103], v[48:51]\n\t"
    "v_mfma_f32_16x16x32_bf16 v[52:55], v[152:155], a[104:107], v[52:55]\n\t"
    "buffer_load_dwordx4 a[216:219], v230, s[52:55], s67 offen offset:0 sc1\n\t"
    "v_mfma_f32_16x16x32_bf16 v[52:55], v[156:159], a[108:111], v[52:55]\n\t"
    "ds_read_b64_tr_b16 v[138:139], v227 offset:33280\n\t"
    "v_mfma_f32_16x16x32_bf16 v[56:59], v[152:155], v[208:211], v[56:59]\n\t"
    "ds_read_b64_tr_b16 v[140:141], v227 offset:36864\n\t"
    "v_mfma_f32_16x16x32_bf16 v[56:59], v[156:159], v[212:215], v[56:59]\n\t"
    "ds_read_b64_tr_b16 v[142:143], v227 offset:37376\n\t"
    "v_mfma_f32_16x16x32_bf16 v[60:63], v[152:155], v[216:219], v[60:63]\n\t"
    "buffer_load_dwordx4 a[220:223], v230, s[52:55], s67 offen offset:1024 sc1\n\t"
    "v_mfma_f32_16x16x32_bf16 v[60:63], v[156:159], v[220:223], v[60:63]\n\t"
    "s_waitcnt vmcnt(16)\n\t"
    "s_barrier\n\t"
    "s_waitcnt vmcnt(28) lgkmcnt(4)\n\t"
    "v_mfma_f32_16x16x32_bf16 v[0:3], v[128:131], a[80:83], v[0:3]\n\t"
    "ds_read_b64_tr_b16 v[144:145], v226 offset:16384\n\t"
    "s_mov_b32 s66, 1572864\n\t"
    "v_mfma_f32_16x16x32_bf16 v[0:3], v[132:135], a[84:87], v[0:3]\n\t"
    "s_add_u32 m0, s64, 114688\n\t"
    "v_mfma_f32_16x16x32_bf16 v[4:7], v[128:131], a[88:91], v[4:7]\n\t"
    "buffer_load_dwordx4 v247, s[48:51], s66 offen lds\n\t"
    "v_mfma_f32_16x16x32_bf16 v[4:7], v[132:135], a[92:95], v[4:7]\n\t"
    "ds_read_b64_tr_b16 v[146:147], v226 offset:16896\n\t"
    "s_waitcnt vmcnt(25)\n\t"
    "v_mfma_f32_16x16x32_bf16 v[8:11], v[128:131], a[240:243], v[8:11]\n\t"
    "ds_read_b64_tr_b16 v[148:149], v226 offset:20480\n\t"
    "s_mov_b32 s66, 1638400\n\t"
    "v_mfma_f32_16x16x32_bf16 v[8:11], v[132:135], a[244:247], v[8:11]\n\t"
    "ds_read_b64_tr_b16 v[150:151], v226 offset:20992\n\t"
    "s_add_u32 m0, s64, 118784\n\t"
    "v_mfma_f32_16x16x32_bf16 v[12:15], v[128:131], a[248:251], v[12:15]\n\t"
    "buffer_load_dwordx4 v247, s[48:51], s66 offen lds\n\t"
    "v_mfma_f32_16x16x32_bf16 v[12:15], v[132:135], a[252:255], v[12:15]\n\t"
    "s_waitcnt lgkmcnt(4)\n\t"
    "v_mfma_f32_16x16x32_bf16 v[16:19], v[136:139], a[80:83], v[16:19]\n\t"
    "ds_read_b64_tr_b16 v[152:153], v227 offset:16384\n\t"
    "s_mov_b32 s66, 1703936\n\t"
    "v_mfma_f32_16x16x32_bf16 v[16:19], v[140:143], a[84:87], v[16:19]\n\t"
    "s_add_u32 m0, s64, 122880\n\t"
    "v_mfma_f32_16x16x32_bf16 v[20:23], v[136:139], a[88:91], v[20:23]\n\t"
    "buffer_load_dwordx4 v233, s[48:51], s66 offen lds\n\t"
    "v_mfma_f32_16x16x32_bf16 v[20:23], v[140:143], a[92:95], v[20:23]\n\t"
    "ds_read_b64_tr_b16 v[154:155], v227 offset:16896\n\t"
    "v_mfma_f32_16x16x32_bf16 v[24:27], v[136:139], a[240:243], v[24:27]\n\t"
    "ds_read_b64_tr_b16 v[156:157], v227 offset:20480\n\t"
    "s_mov_b32 s66, 1769472\n\t"
    "v_mfma_f32_16x16x32_bf16 v[24:27], v[140:143], a[244:247], v[24:27]\n\t"
    "ds_read_b64_tr_b16 v[158:159], v227 offset:20992\n\t"
    "s_add_u32 m0, s64, 126976\n\t"
    "v_mfma_f32_16x16x32_bf16 v[28:31], v[136:139], a[248:251], v[28:31]\n\t"
    "buffer_load_dwordx4 v233, s[48:51], s66 offen lds\n\t"
    "v_mfma_f32_16x16x32_bf16 v[28:31], v[140:143], a[252:255], v[28:31]\n\t"
    "s_waitcnt vmcnt(16) lgkmcnt(4)\n\t"
    "v_mfma_f32_16x16x32_bf16 v[0:3], v[144:147], a[64:67], v[0:3]\n\t"
    "ds_read_b64_tr_b16 v[128:129], v228 offset:0\n\t"
    "s_mov_b32 s66, 1835008\n\t"
    "v_mfma_f32_16x16x32_bf16 v[0:3], v[148:151], a[68:71], v[0:3]\n\t"
    "s_add_u32 m0, s64, 147456\n\t"
    "v_mfma_f32_16x16x32_bf16 v[4:7], v[144:147], a[72:75], v[4:7]\n\t"
    "buffer_load_dwordx4 v247, s[48:51], s66 offen lds\n\t"
    "v_mfma_f32_16x16x32_bf16 v[4:7], v[148:151], a[76:79], v[4:7]\n\t"
    "ds_read_b64_tr_b16 v[130:131], v228 offset:512\n\t"
    "s_waitcnt vmcnt(13)\n\t"
    "v_mfma_f32_16x16x32_bf16 v[8:11], v[144:147], a[224:227], v[8:11]\n\t"
    "ds_read_b64_tr_b16 v[132:133], v228 offset:4096\n\t"
    "s_mov_b32 s66, 1900544\n\t"
    "v_mfma_f32_16x16x32_bf16 v[8:11], v[148:151], a[228:231], v[8:11]\n\t"
    "ds_read_b64_tr_b16 v[134:135], v228 offset:4608\n\t"
    "s_add_u32 m0, s64, 151552\n\t"
    "v_mfma_f32_16x16x32_bf16 v[12:15], v[144:147], a[232:235], v[12:15]\n\t"
    "buffer_load_dwordx4 v247, s[48:51], s66 offen lds\n\t"
    "v_mfma_f32_16x16x32_bf16 v[12:15], v[148:151], a[236:239], v[12:15]\n\t"
    "s_waitcnt lgkmcnt(4)\n\t"
    "v_mfma_f32_16x16x32_bf16 v[16:19], v[152:155], a[64:67], v[16:19]\n\t"
    "ds_read_b64_tr_b16 v[136:137], v229 offset:0\n\t"
    "s_mov_b32 s66, 1966080\n\t"
    "v_mfma_f32_16x16x32_bf16 v[16:19], v[156:159], a[68:71], v[16:19]\n\t"
    "s_add_u32 m0, s64, 155648\n\t"
    "v_mfma_f32_16x16x32_bf16 v[20:23], v[152:155], a[72:75], v[20:23]\n\t"
    "buffer_load_dwordx4 v233, s[48:51], s66 offen lds\n\t"
    "v_mfma_f32_16x16x32_bf16 v[20:23], v[156:159], a[76:79], v[20:23]\n\t"
    "ds_read_b64_tr_b16 v[138:139], v229 offset:512\n\t"
    "v_mfma_f32_16x16x32_bf16 v[24:27], v[152:155], a[224:227], v[24:27]\n\t"
    "ds_read_b64_tr_b16 v[140:141], v229 offset:4096\n\t"
    "s_mov_b32 s66, 2031616\n\t"
    "v_mfma_f32_16x16x32_bf16 v[24:27], v[156:159], a[228:231], v[24:27]\n\t"
    "ds_read_b64_tr_b16 v[142:143], v229 offset:4608\n\t"
    "s_add_u32 m0, s64, 159744\n\t"
    "v_mfma_f32_16x16x32_bf16 v[28:31], v[152:155], a[232:235], v[28:31]\n\t"
    "buffer_load_dwordx4 v233, s[48:51], s66 offen lds\n\t"
    "v_mfma_f32_16x16x32_bf16 v[28:31], v[156:159], a[236:239], v[28:31]\n\t"
    "s_waitcnt vmcnt(12) lgkmcnt(4)\n\t"
    "v_mfma_f32_16x16x32_bf16 v[0:3], v[128:131], a[48:51], v[0:3]\n\t"
    "ds_read_b64_tr_b16 v[144:145], v226 offset:33024\n\t"
    "s_mov_b32 s66, 2097152\n\t"
    "ST6_%=:\n\t"
    "s_cbranch_execz ST10_%=\n\t"
    "v_mfma_f32_16x16x32_bf16 v[0:3], v[132:135], a[52:55], v[0:3]\n\t"
    "s_add_u32 m0, s64, 65536\n\t"
    "v_mfma_f32_16x16x32_bf16 v[4:7], v[128:131], a[56:59], v[4:7]\n\t"
    "buffer_load_dwordx4 v247, s[48:51], s66 offen lds\n\t"
    "v_mfma_f32_16x16x32_bf16 v[4:7], v[132:135], a[60:63], v[4:7]\n\t"
    "ds_read_b64_tr_b16 v[146:147], v226 offset:33536\n\t"
    "s_waitcnt vmcnt(9)\n\t"
    "v_mfma_f32_16x16x32_bf16 v[8:11], v[128:131], a[208:211], v[8:11]\n\t"
    "ds_read_b64_tr_b16 v[148:149], v226 offset:37120\n\t"
    "s_mov_b32 s66, 2162688\n\t"
    "v_mfma_f32_16x16x32_bf16 v[8:11], v[132:135], a[212:215], v[8:11]\n\t"
    "ds_read_b64_tr_b16 v[150:151], v226 offset:37632\n\t"
    "s_add_u32 m0, s64, 69632\n\t"
    "v_mfma_f32_16x16x32_bf16 v[12:15], v[128:131], a[216:219], v[12:15]\n\t"
    "buffer_load_dwordx4 v247, s[48:51], s66 offen lds\n\t"
    "v_mfma_f32_16x16x32_bf16 v[12:15], v[132:135], a[220:223], v[12:15]\n\t"
    "s_waitcnt lgkmcnt(4)\n\t"
    "v_mfma_f32_16x16x32_bf16 v[16:19], v[136:139], a[48:51], v[16:19]\n\t"
    "ds_read_b64_tr_b16 v[152:153], v227 offset:33024\n\t"
    "s_mov_b32 s66, 2228224\n\t"
    "v_mfma_f32_16x16x32_bf16 v[16:19], v[140:143], a[52:55], v[16:19]\n\t"
    "s_add_u32 m0, s64, 73728\n\t"
    "v_mfma_f32_16x16x32_bf16 v[20:23], v[136:139], a[56:59], v[20:23]\n\t"
    "buffer_load_dwordx4 v233, s[48:51], s66 offen lds\n\t"
    "v_mfma_f32_16x16x32_bf16 v[20:23], v[140:143], a[60:63], v[20:23]\n\t"
    "ds_read_b64_tr_b16 v[154:155], v227 offset:33536\n\t"
    "v_mfma_f32_16x16x32_bf16 v[24:27], v[136:139], a[208:211], v[24:27]\n\t"
    "ds_read_b64_tr_b16 v[156:157], v227 offset:37120\n\t"
    "s_mov_b32 s66, 2293760\n\t"
    "v_mfma_f32_16x16x32_bf16 v[24:27], v[140:143], a[212:215], v[24:27]\n\t"
    "ds_read_b64_tr_b16 v[158:159], v227 offset:37632\n\t"
    "s_add_u32 m0, s64, 77824\n\t"
    "v_mfma_f32_16x16x32_bf16 v[28:31], v[136:139], a[216:219], v[28:31]\n\t"
    "buffer_load_dwordx4 v233, s[48:51], s66 offen lds\n\t"
    "v_mfma_f32_16x16x32_bf16 v[28:31], v[140:143], a[220:223], v[28:31]\n\t"
    "s_waitcnt lgkmcnt(4)\n\t"
    "v_mfma_f32_16x16x32_bf16 v[32:35], v[144:147], a[80:83], v[32:35]\n\t"
    "ds_read_b64_tr_b16 v[128:129], v226 offset:16640\n\t"
    "s_mov_b32 s66, 2359296\n\t"
    "v_mfma_f32_16x16x32_bf16 v[32:35], v[148:151], a[84:87], v[32:35]\n\t"
    "s_add_u32 m0, s64, 32768\n\t"
    "v_mfma_f32_16x16x32_bf16 v[36:39], v[144:147], a[88:91], v[36:39]\n\t"
    "buffer_load_dwordx4 v247, s[48:51], s66 offen lds\n\t"
    "s_mov_b32 s71, 16384\n\t"
    "s_cmp_gt_u32 s68, 1\n\t"
    "s_cselect_b32 s71, s71, 0\n\t"
    "v_and_b32 v238, 0x3fff, v253\n\t"
    "v_add_u32 v238, s71, v238\n\t"
    "v_mfma_f32_16x16x32_bf16 v[36:39], v[148:151], a[92:95], v[36:39]\n\t"
    "ds_read_b64_tr_b16 v[130:131], v226 offset:17152\n\t"
    "v_mfma_f32_16x16x32_bf16 v[40:43], v[144:147], a[240:243], v[40:43]\n\t"
    "ds_read_b64_tr_b16 v[132:133], v226 offset:20736\n\t"
    "s_mov_b32 s66, 2424832\n\t"
    "v_xor_b32 v239, 16384, v238\n\t"
    "v_mfma_f32_16x16x32_bf16 v[40:43], v[148:151], a[244:247], v[40:43]\n\t"
    "ds_read_b64_tr_b16 v[134:135], v226 offset:21248\n\t"
    "s_add_u32 m0, s64, 36864\n\t"
    "v_mfma_f32_16x16x32_bf16 v[44:47], v[144:147], a[248:251], v[44:47]\n\t"
    "buffer_load_dwordx4 v247, s[48:51], s66 offen lds\n\t"
    "ds_write_b128 v238, v[0:3] offset:0\n\t"
    "v_mfma_f32_16x16x32_bf16 v[44:47], v[148:151], a[252:255], v[44:47]\n\t"
    "s_waitcnt lgkmcnt(5)\n\t"
    "v_mfma_f32_16x16x32_bf16 v[48:51], v[152:155], a[80:83], v[48:51]\n\t"
    "ds_read_b64_tr_b16 v[136:137], v227 offset:16640\n\t"
    "s_mov_b32 s66, 2490368\n\t"
    "ds_write_b128 v238, v[4:7] offset:1024\n\t"
    "v_mfma_f32_16x16x32_bf16 v[48:51], v[156:159], a[84:87], v[48:51]\n\t"
    "s_add_u32 m0, s64, 40960\n\t"
    "v_mfma_f32_16x16x32_bf16 v[52:55], v[152:155], a[88:91], v[52:55]\n\t"
    "buffer_load_dwordx4 v233, s[48:51], s66 offen lds\n\t"
    "ds_write_b128 v238, v[8:11] offset:2048\n\t"
    "v_mfma_f32_16x16x32_bf16 v[52:55], v[156:159], a[92:95], v[52:55]\n\t"
    "ds_read_b64_tr_b16 v[138:139], v227 offset:17152\n\t"
    "v_mfma_f32_16x16x32_bf16 v[56:59], v[152:155], a[240:243], v[56:59]\n\t"
    "ds_read_b64_tr_b16 v[140:141], v227 offset:20736\n\t"
    "s_mov_b32 s66, 2555904\n\t"
    "ds_write_b128 v238, v[12:15] offset:3072\n\t"
    "v_mfma_f32_16x16x32_bf16 v[56:59], v[156:159], a[244:247], v[56:59]\n\t"
    "ds_read_b64_tr_b16 v[142:143], v227 offset:21248\n\t"
    "s_add_u32 m0, s64, 45056\n\t"
    "v_mfma_f32_16x16x32_bf16 v[60:63], v[152:155], a[248:251], v[60:63]\n\t"
    "buffer_load_dwordx4 v233, s[48:51], s66 offen lds\n\t"
    "ds_write_b128 v238, v[16:19] offset:4096\n\t"
    "v_mfma_f32_16x16x32_bf16 v[60:63], v[156:159], a[252:255], v[60:63]\n\t"
    "s_waitcnt lgkmcnt(9)\n\t"
    "v_mfma_f32_16x16x32_bf16 v[32:35], v[128:131], a[64:67], v[32:35]\n\t"
    "ds_read_b64_tr_b16 v[144:145], v228 offset:256\n\t"
    "s_mov_b32 s66, 2621440\n\t"
    "ds_write_b128 v238, v[20:23] offset:5120\n\t"
    "v_mfma_f32_16x16x32_bf16 v[32:35], v[132:135], a[68:71], v[32:35]\n\t"
    "s_add_u32 m0, s64, 49152\n\t"
    "v_mfma_f32_16x16x32_bf16 v[36:39], v[128:131], a[72:75], v[36:39]\n\t"
    "buffer_load_dwordx4 v247, s[48:51], s66 offen lds\n\t"
    "ds_write_b128 v238, v[24:27] offset:6144\n\t"
    "v_mfma_f32_16x16x32_bf16 v[36:39], v[132:135], a[76:79], v[36:39]\n\t"
    "ds_read_b64_tr_b16 v[146:147], v228 offset:768\n\t"
    "v_mfma_f32_16x16x32_bf16 v[40:43], v[128:131], a[224:227], v[40:43]\n\t"
    "ds_read_b64_tr_b16 v[148:149], v228 offset:4352\n\t"
    "s_mov_b32 s66, 2686976\n\t"
    "ds_write_b128 v238, v[28:31] offset:7168\n\t"
    "v_mfma_f32_16x16x32_bf16 v[40:43], v[132:135], a[228:231], v[40:43]\n\t"
    "ds_read_b64_tr_b16 v[150:151], v228 offset:4864\n\t"
    "s_add_u32 m0, s64, 53248\n\t"
    "v_mfma_f32_16x16x32_bf16 v[44:47], v[128:131], a[232:235], v[44:47]\n\t"
    "buffer_load_dwordx4 v247, s[48:51], s66 offen lds\n\t"
    "v_mfma_f32_16x16x32_bf16 v[44:47], v[132:135], a[236:239], v[44:47]\n\t"
    "s_waitcnt lgkmcnt(8)\n\t"
    "v_mfma_f32_16x16x32_bf16 v[48:51], v[136:139], a[64:67], v[48:51]\n\t"
    "ds_read_b64_tr_b16 v[152:153], v229 offset:256\n\t"
    "s_mov_b32 s66, 2752512\n\t"
    "v_mfma_f32_16x16x32_bf16 v[48:51], v[140:143], a[68:71], v[48:51]\n\t"
    "s_add_u32 m0, s64, 57344\n\t"
    "v_mfma_f32_16x16x32_bf16 v[52:55], v[136:139], a[72:75], v[52:55]\n\t"
    "buffer_load_dwordx4 v233, s[48:51], s66 offen lds\n\t"
    "v_mfma_f32_16x16x32_bf16 v[52:55], v[140:143], a[76:79], v[52:55]\n\t"
    "ds_read_b64_tr_b16 v[154:155], v229 offset:768\n\t"
    "v_mfma_f32_16x16x32_bf16 v[56:59], v[136:139], a[224:227], v[56:59]\n\t"
    "ds_read_b64_tr_b16 v[156:157], v229 offset:4352\n\t"
    "s_mov_b32 s66, 2818048\n\t"
    "v_mfma_f32_16x16x32_bf16 v[56:59], v[140:143], a[228:231], v[56:59]\n\t"
    "ds_read_b64_tr_b16 v[158:159], v229 offset:4864\n\t"
    "s_add_u32 m0, s64, 61440\n\t"
    "v_mfma_f32_16x16x32_bf16 v[60:63], v[136:139], a[232:235], v[60:63]\n\t"
    "buffer_load_dwordx4 v233, s[48:51], s66 offen lds\n\t"
    "v_mfma_f32_16x16x32_bf16 v[60:63], v[140:143], a[236:239], v[60:63]\n\t"
    "s_waitcnt vmcnt(12) lgkmcnt(5)\n\t"
    "s_barrier\n\t"
    "s_waitcnt lgkmcnt(4)\n\t"
    "v_mfma_f32_16x16x32_bf16 v[32:35], v[144:147], a[48:51], v[32:35]\n\t"
    "ds_read_b64_tr_b16 v[128:129], v226 offset:49152\n\t"
    "s_mov_b32 s66, 2883584\n\t"
    "ds_read_b128 v[0:3], v239 offset:0\n\t"
    "v_mfma_f32_16x16x32_bf16 v[32:35], v[148:151], a[52:55], v[32:35]\n\t"
    "s_add_u32 m0, s64, 98304\n\t"
    "ds_read_b128 v[4:7], v239 offset:1024\n\t"
    "v_mfma_f32_16x16x32_bf16 v[36:39], v[144:147], a[56:59], v[36:39]\n\t"
    "buffer_load_dwordx4 v247, s[48:51], s66 offen lds\n\t"
    "ds_read_b128 v[8:11], v239 offset:2048\n\t"
    "v_mfma_f32_16x16x32_bf16 v[36:39], v[148:151], a[60:63], v[36:39]\n\t"
    "ds_read_b64_tr_b16 v[130:131], v226 offset:49664\n\t"
    "ds_read_b128 v[12:15], v239 offset:3072\n\t"
    "v_mfma_f32_16x16x32_bf16 v[40:43], v[144:147], a[208:211], v[40:43]\n\t"
    "ds_read_b64_tr_b16 v[132:133], v226 offset:53248\n\t"
    "s_mov_b32 s66, 2949120\n\t"
    "ds_read_b128 v[16:19], v239 offset:4096\n\t"
    "v_mfma_f32_16x16x32_bf16 v[40:43], v[148:151], a[212:215], v[40:43]\n\t"
    "ds_read_b64_tr_b16 v[134:135], v226 offset:53760\n\t"
    "s_add_u32 m0, s64, 102400\n\t"
    "ds_read_b128 v[20:23], v239 offset:5120\n\t"
    "v_mfma_f32_16x16x32_bf16 v[44:47], v[144:147], a[216:219], v[44:47]\n\t"
    "buffer_load_dwordx4 v247, s[48:51], s66 offen lds\n\t"
    "ds_read_b128 v[24:27], v239 offset:6144\n\t"
    "v_mfma_f32_16x16x32_bf16 v[44:47], v[148:151], a[220:223], v[44:47]\n\t"
    "ds_read_b128 v[28:31], v239 offset:7168\n\t"
    "s_waitcnt lgkmcnt(12)\n\t"
    "v_mfma_f32_16x16x32_bf16 v[48:51], v[152:155], a[48:51], v[48:51]\n\t"
    "ds_read_b64_tr_b16 v[136:137], v227 offset:49152\n\t"
    "s_mov_b32 s66, 3014656\n\t"
    "v_mfma_f32_16x16x32_bf16 v[48:51], v[156:159], a[52:55], v[48:51]\n\t"
    "s_add_u32 m0, s64, 106496\n\t"
    "v_mfma_f32_16x16x32_bf16 v[52:55], v[152:155], a[56:59], v[52:55]\n\t"
    "buffer_load_dwordx4 v233, s[48:51], s66 offen lds\n\t"
    "v_mfma_f32_16x16x32_bf16 v[52:55], v[156:159], a[60:63], v[52:55]\n\t"
    "ds_read_b64_tr_b16 v[138:139], v227 offset:49664\n\t"
    "v_mfma_f32_16x16x32_bf16 v[56:59], v[152:155], a[208:211], v[56:59]\n\t"
    "ds_read_b64_tr_b16 v[140:141], v227 offset:53248\n\t"
    "s_mov_b32 s66, 3080192\n\t"
    "v_mfma_f32_16x16x32_bf16 v[56:59], v[156:159], a[212:215], v[56:59]\n\t"
    "ds_read_b64_tr_b16 v[142:143], v227 offset:53760\n\t"
    "s_add_u32 m0, s64, 110592\n\t"
    "v_mfma_f32_16x16x32_bf16 v[60:63], v[152:155], a[216:219], v[60:63]\n\t"
    "buffer_load_dwordx4 v233, s[48:51], s66 offen lds\n\t"
    "v_mfma_f32_16x16x32_bf16 v[60:63], v[156:159], a[220:223], v[60:63]\n\t"
    "s_waitcnt lgkmcnt(7)\n\t"
    "v_mfma_f32_16x16x32_bf16 v[64:67], v[128:131], a[160:163], v[64:67]\n\t"
    "ds_read_b64_tr_b16 v[144:145], v226 offset:49408\n\t"
    "s_mov_b32 s66, 3145728\n\t"
    "v_mfma_f32_16x16x32_bf16 v[64:67], v[132:135], a[164:167], v[64:67]\n\t"
    "s_add_u32 m0, s64, 81920\n\t"
    "v_mfma_f32_16x16x32_bf16 v[68:71], v[128:131], a[168:171], v[68:71]\n\t"
    "buffer_load_dwordx4 v247, s[48:51], s66 offen lds\n\t"
    "v_mfma_f32_16x16x32_bf16 v[68:71], v[132:135], a[172:175], v[68:71]\n\t"
    "ds_read_b64_tr_b16 v[146:147], v226 offset:49920\n\t"
    "v_mfma_f32_16x16x32_bf16 v[72:75], v[128:131], a[0:3], v[72:75]\n\t"
    "ds_read_b64_tr_b16 v[148:149], v226 offset:53504\n\t"
    "s_mov_b32 s66, 3211264\n\t"
    "v_mfma_f32_16x16x32_bf16 v[72:75], v[132:135], a[4:7], v[72:75]\n\t"
    "ds_read_b64_tr_b16 v[150:151], v226 offset:54016\n\t"
    "s_add_u32 m0, s64, 86016\n\t"
    "v_mfma_f32_16x16x32_bf16 v[76:79], v[128:131], a[8:11], v[76:79]\n\t"
    "buffer_load_dwordx4 v247, s[48:51], s66 offen lds\n\t"
    "v_mfma_f32_16x16x32_bf16 v[76:79], v[132:135], a[12:15], v[76:79]\n\t"
    "s_waitcnt lgkmcnt(4)\n\t"
    "v_mfma_f32_16x16x32_bf16 v[80:83], v[136:139], a[160:163], v[80:83]\n\t"
    "ds_read_b64_tr_b16 v[152:153], v227 offset:49408\n\t"
    "s_waitcnt lgkmcnt(9)\n\t"
    "v_add_f32 v32, v32, v0\n\t"
    "v_mfma_f32_16x16x32_bf16 v[80:83], v[140:143], a[164:167], v[80:83]\n\t"
    "v_add_f32 v33, v33, v1\n\t"
    "v_min_u32 v32, -2, v32\n\t"
    "v_mfma_f32_16x16x32_bf16 v[84:87], v[136:139], a[168:171], v[84:87]\n\t"
    "v_add_f32 v34, v34, v2\n\t"
    "v_min_u32 v33, -2, v33\n\t"
    "v_mfma_f32_16x16x32_bf16 v[84:87], v[140:143], a[172:175], v[84:87]\n\t"
    "ds_read_b64_tr_b16 v[154:155], v227 offset:49920\n\t"
    "v_add_f32 v35, v35, v3\n\t"
    "v_min_u32 v34, -2, v34\n\t"
    "v_mfma_f32_16x16x32_bf16 v[88:91], v[136:139], a[0:3], v[88:91]\n\t"
    "ds_read_b64_tr_b16 v[156:157], v227 offset:53504\n\t"
    "v_add_f32 v36, v36, v4\n\t"
    "v_min_u32 v35, -2, v35\n\t"
    "v_mfma_f32_16x16x32_bf16 v[88:91], v[140:143], a[4:7], v[88:91]\n\t"
    "ds_read_b64_tr_b16 v[158:159], v227 offset:54016\n\t"
    "v_add_f32 v37, v37, v5\n\t"
    "v_min_u32 v36, -2, v36\n\t"
    "s_mov_b32 s67, 4096\n\t"
    "v_mfma_f32_16x16x32_bf16 v[92:95], v[136:139], a[8:11], v[92:95]\n\t"
    "v_add_f32 v38, v38, v6\n\t"
    "v_min_u32 v37, -2, v37\n\t"
    "buffer_store_dwordx4 v[32:35], v253, s[52:55], 0 offen offset:0 sc1\n\t"
    "v_mfma_f32_16x16x32_bf16 v[92:95], v[140:143], a[12:15], v[92:95]\n\t"
    "v_add_f32 v39, v39, v7\n\t"
    "v_min_u32 v38, -2, v38\n\t"
    "s_waitcnt lgkmcnt(4)\n\t"
    "v_mfma_f32_16x16x32_bf16 v[96:99], v[144:147], a[160:163], v[96:99]\n\t"
    "ds_read_b64_tr_b16 v[128:129], v228 offset:16384\n\t"
    "v_add_f32 v40, v40, v8\n\t"
    "v_min_u32 v39, -2, v39\n\t"
    "v_mfma_f32_16x16x32_bf16 v[96:99], v[148:151], a[164:167], v[96:99]\n\t"
    "v_add_f32 v41, v41, v9\n\t"
    "v_min_u32 v40, -2, v40\n\t"
    "v_mfma_f32_16x16x32_bf16 v[100:103], v[144:147], a[168:171], v[100:103]\n\t"
    "v_add_f32 v42, v42, v10\n\t"
    "v_min_u32 v41, -2, v41\n\t"
    "buffer_store_dwordx4 v[36:39], v253, s[52:55], 0 offen offset:1024 sc1\n\t"
    "v_mfma_f32_16x16x32_bf16 v[100:103], v[148:151], a[172:175], v[100:103]\n\t"
    "ds_read_b64_tr_b16 v[130:131], v228 offset:16896\n\t"
    "v_add_f32 v43, v43, v11\n\t"
    "v_min_u32 v42, -2, v42\n\t"
    "v_mfma_f32_16x16x32_bf16 v[104:107], v[144:147], a[0:3], v[104:107]\n\t"
    "ds_read_b64_tr_b16 v[132:133], v228 offset:20480\n\t"
    "v_add_f32 v44, v44, v12\n\t"
    "v_min_u32 v43, -2, v43\n\t"
    "v_mfma_f32_16x16x32_bf16 v[104:107], v[148:151], a[4:7], v[104:107]\n\t"
    "ds_read_b64_tr_b16 v[134:135], v228 offset:20992\n\t"
    "v_add_f32 v45, v45, v13\n\t"
    "v_min_u32 v44, -2, v44\n\t"
    "v_mfma_f32_16x16x32_bf16 v[108:111], v[144:147], a[8:11], v[108:111]\n\t"
    "v_add_f32 v46, v46, v14\n\t"
    "v_min_u32 v45, -2, v45\n\t"
    "buffer_store_dwordx4 v[40:43], v253, s[52:55], 0 offen offset:2048 sc1\n\t"
    "v_mfma_f32_16x16x32_bf16 v[108:111], v[148:151], a[12:15], v[108:111]\n\t"
    "v_add_f32 v47, v47, v15\n\t"
    "v_min_u32 v46, -2, v46\n\t"
    "s_waitcnt lgkmcnt(4)\n\t"
    "v_mfma_f32_16x16x32_bf16 v[112:115], v[152:155], a[160:163], v[112:115]\n\t"
    "ds_read_b64_tr_b16 v[136:137], v229 offset:16384\n\t"
    "v_add_f32 v48, v48, v16\n\t"
    "v_min_u32 v47, -2, v47\n\t"
    "v_mfma_f32_16x16x32_bf16 v[112:115], v[156:159], a[164:167], v[112:115]\n\t"
    "v_add_f32 v49, v49, v17\n\t"
    "v_min_u32 v48, -2, v48\n\t"
    "v_mfma_f32_16x16x32_bf16 v[116:119], v[152:155], a[168:171], v[116:119]\n\t"
    "v_add_f32 v50, v50, v18\n\t"
    "v_min_u32 v49, -2, v49\n\t"
    "buffer_store_dwordx4 v[44:47], v253, s[52:55], 0 offen offset:3072 sc1\n\t"
    "v_mfma_f32_16x16x32_bf16 v[116:119], v[156:159], a[172:175], v[116:119]\n\t"
    "ds_read_b64_tr_b16 v[138:139], v229 offset:16896\n\t"
    "v_add_f32 v51, v51, v19\n\t"
    "v_min_u32 v50, -2, v50\n\t"
    "v_mfma_f32_16x16x32_bf16 v[120:123], v[152:155], a[0:3], v[120:123]\n\t"
    "ds_read_b64_tr_b16 v[140:141], v229 offset:20480\n\t"
    "v_add_f32 v52, v52, v20\n\t"
    "v_min_u32 v51, -2, v51\n\t"
    "v_mfma_f32_16x16x32_bf16 v[120:123], v[156:159], a[4:7], v[120:123]\n\t"
    "ds_read_b64_tr_b16 v[142:143], v229 offset:20992\n\t"
    "v_add_f32 v53, v53, v21\n\t"
    "v_min_u32 v52, -2, v52\n\t"
    "v_mfma_f32_16x16x32_bf16 v[124:127], v[152:155], a[8:11], v[124:127]\n\t"
    "v_add_f32 v54, v54, v22\n\t"
    "v_min_u32 v53, -2, v53\n\t"
    "buffer_store_dwordx4 v[48:51], v253, s[52:55], s67 offen offset:0 sc1\n\t"
    "v_mfma_f32_16x16x32_bf16 v[124:127], v[156:159], a[12:15], v[124:127]\n\t"
    "v_add_f32 v55, v55, v23\n\t"
    "v_min_u32 v54, -2, v54\n\t"
    "s_waitcnt lgkmcnt(4)\n\t"
    "v_mfma_f32_16x16x32_bf16 v[64:67], v[128:131], a[144:147], v[64:67]\n\t"
    "ds_read_b64_tr_b16 v[144:145], v228 offset:16640\n\t"
    "v_add_f32 v56, v56, v24\n\t"
    "v_min_u32 v55, -2, v55\n\t"
    "v_mfma_f32_16x16x32_bf16 v[64:67], v[132:135], a[148:151], v[64:67]\n\t"
    "v_add_f32 v57, v57, v25\n\t"
    "v_min_u32 v56, -2, v56\n\t"
    "v_mfma_f32_16x16x32_bf16 v[68:71], v[128:131], a[152:155], v[68:71]\n\t"
    "v_add_f32 v58, v58, v26\n\t"
    "v_min_u32 v57, -2, v57\n\t"
    "buffer_store_dwordx4 v[52:55], v253, s[52:55], s67 offen offset:1024 sc1\n\t"
    "v_mfma_f32_16x16x32_bf16 v[68:71], v[132:135], a[156:159], v[68:71]\n\t"
    "ds_read_b64_tr_b16 v[146:147], v228 offset:17152\n\t"
    "v_add_f32 v59, v59, v27\n\t"
    "v_min_u32 v58, -2, v58\n\t"
    "v_mfma_f32_16x16x32_bf16 v[72:75], v[128:131], v[160:163], v[72:75]\n\t"
    "ds_read_b64_tr_b16 v[148:149], v228 offset:20736\n\t"
    "v_add_f32 v60, v60, v28\n\t"
    "v_min_u32 v59, -2, v59\n\t"
    "v_mfma_f32_16x16x32_bf16 v[72:75], v[132:135], v[164:167], v[72:75]\n\t"
    "ds_read_b64_tr_b16 v[150:151], v228 offset:21248\n\t"
    "v_add_f32 v61, v61, v29\n\t"
    "v_min_u32 v60, -2, v60\n\t"
    "v_mfma_f32_16x16x32_bf16 v[76:79], v[128:131], v[168:171], v[76:79]\n\t"
    "v_add_f32 v62, v62, v30\n\t"
    "v_min_u32 v61, -2, v61\n\t"
    "buffer_store_dwordx4 v[56:59], v253, s[52:55], s67 offen offset:2048 sc1\n\t"
    "v_mfma_f32_16x16x32_bf16 v[76:79], v[132:135], v[172:175], v[76:79]\n\t"
    "v_add_f32 v63, v63, v31\n\t"
    "v_min_u32 v62, -2, v62\n\t"
    "s_waitcnt lgkmcnt(4)\n\t"
    "v_mfma_f32_16x16x32_bf16 v[80:83], v[136:139], a[144:147], v[80:83]\n\t"
    "ds_read_b64_tr_b16 v[152:153], v229 offset:16640\n\t"
    "v_min_u32 v63, -2, v63\n\t"
    "v_mfma_f32_16x16x32_bf16 v[80:83], v[140:143], a[148:151], v[80:83]\n\t"
    "v_mfma_f32_16x16x32_bf16 v[84:87], v[136:139], a[152:155], v[84:87]\n\t"
    "buffer_store_dwordx4 v[60:63], v253, s[52:55], s67 offen offset:3072 sc1\n\t"
    "v_mfma_f32_16x16x32_bf16 v[84:87], v[140:143], a[156:159], v[84:87]\n\t"
    "ds_read_b64_tr_b16 v[154:155], v229 offset:17152\n\t"
    "v_mfma_f32_16x16x32_bf16 v[88:91], v[136:139], v[160:163], v[88:91]\n\t"
    "ds_read_b64_tr_b16 v[156:157], v229 offset:20736\n\t"
    "s_mov_b32 s66, 3276800\n\t"
    "v_mfma_f32_16x16x32_bf16 v[88:91], v[140:143], v[164:167], v[88:91]\n\t"
    "ds_read_b64_tr_b16 v[158:159], v229 offset:21248\n\t"
    "s_add_u32 m0, s64, 90112\n\t"
    "v_mfma_f32_16x16x32_bf16 v[92:95], v[136:139], v[168:171], v[92:95]\n\t"
    "buffer_load_dwordx4 v233, s[48:51], s66 offen lds\n\t"
    "v_mfma_f32_16x16x32_bf16 v[92:95], v[140:143], v[172:175], v[92:95]\n\t"
    "s_waitcnt vmcnt(19)\n\t"
    "s_barrier\n\t"
    "s_waitcnt lgkmcnt(4)\n\t"
    "v_mfma_f32_16x16x32_bf16 v[96:99], v[144:147], a[144:147], v[96:99]\n\t"
    "ds_read_b64_tr_b16 v[128:129], v226 offset:0\n\t"
    "s_mov_b32 s66, 3342336\n\t"
    "v_mfma_f32_16x16x32_bf16 v[96:99], v[148:151], a[148:151], v[96:99]\n\t"
    "s_add_u32 m0, s64, 94208\n\t"
    "v_mfma_f32_16x16x32_bf16 v[100:103], v[144:147], a[152:155], v[100:103]\n\t"
    "buffer_load_dwordx4 v233, s[48:51], s66 offen lds\n\t"
    "v_mfma_f32_16x16x32_bf16 v[100:103], v[148:151], a[156:159], v[100:103]\n\t"
    "ds_read_b64_tr_b16 v[130:131], v226 offset:512\n\t"
    "v_mfma_f32_16x16x32_bf16 v[104:107], v[144:147], v[160:163], v[104:107]\n\t"
    "ds_read_b64_tr_b16 v[132:133], v226 offset:4096\n\t"
    "s_mov_b32 s66, 3407872\n\t"
    "v_mfma_f32_16x16x32_bf16 v[104:107], v[148:151], v[164:167], v[104:107]\n\t"
    "ds_read_b64_tr_b16 v[134:135], v226 offset:4608\n\t"
    "s_add_u32 m0, s64, 131072\n\t"
    "v_mfma_f32_16x16x32_bf16 v[108:111], v[144:147], v[168:171], v[108:111]\n\t"
    "buffer_load_dwordx4 v247, s[48:51], s66 offen lds\n\t"
    "v_mfma_f32_16x16x32_bf16 v[108:111], v[148:151], v[172:175], v[108:111]\n\t"
    "s_waitcnt lgkmcnt(4)\n\t"
    "v_mfma_f32_16x16x32_bf16 v[112:115], v[152:155], a[144:147], v[112:115]\n\t"
    "ds_read_b64_tr_b16 v[136:137], v227 offset:0\n\t"
    "s_mov_b32 s66, 3473408\n\t"
    "v_mfma_f32_16x16x32_bf16 v[112:115], v[156:159], a[148:151], v[112:115]\n\t"
    "s_add_u32 m0, s64, 135168\n\t"
    "v_mfma_f32_16x16x32_bf16 v[116:119], v[152:155], a[152:155], v[116:119]\n\t"
    "buffer_load_dwordx4 v247, s[48:51], s66 offen lds\n\t"
    "v_mfma_f32_16x16x32_bf16 v[116:119], v[156:159], a[156:159], v[116:119]\n\t"
    "ds_read_b64_tr_b16 v[138:139], v227 offset:512\n\t"
    "v_mfma_f32_16x16x32_bf16 v[120:123], v[152:155], v[160:163], v[120:123]\n\t"
    "ds_read_b64_tr_b16 v[140:141], v227 offset:4096\n\t"
    "s_mov_b32 s66, 3538944\n\t"
    "v_mfma_f32_16x16x32_bf16 v[120:123], v[156:159], v[164:167], v[120:123]\n\t"
    "ds_read_b64_tr_b16 v[142:143], v227 offset:4608\n\t"
    "s_add_u32 m0, s64, 139264\n\t"
    "v_mfma_f32_16x16x32_bf16 v[124:127], v[152:155], v[168:171], v[124:127]\n\t"
    "buffer_load_dwordx4 v233, s[48:51], s66 offen lds\n\t"
    "v_mfma_f32_16x16x32_bf16 v[124:127], v[156:159], v[172:175], v[124:127]\n\t"
    "s_waitcnt lgkmcnt(4)\n\t"
    "v_mfma_f32_16x16x32_bf16 v[64:67], v[128:131], a[128:131], v[64:67]\n\t"
    "ds_read_b64_tr_b16 v[144:145], v226 offset:256\n\t"
    "s_mov_b32 s66, 3604480\n\t"
    "v_mfma_f32_16x16x32_bf16 v[64:67], v[132:135], a[132:135], v[64:67]\n\t"
    "s_add_u32 m0, s64, 143360\n\t"
    "v_mfma_f32_16x16x32_bf16 v[68:71], v[128:131], a[136:139], v[68:71]\n\t"
    "buffer_load_dwordx4 v233, s[48:51], s66 offen lds\n\t"
    "v_mfma_f32_16x16x32_bf16 v[68:71], v[132:135], a[140:143], v[68:71]\n\t"
    "ds_read_b64_tr_b16 v[146:147], v226 offset:768\n\t"
    "v_mfma_f32_16x16x32_bf16 v[72:75], v[128:131], v[176:179], v[72:75]\n\t"
    "ds_read_b64_tr_b16 v[148:149], v226 offset:4352\n\t"
    "v_mfma_f32_16x16x32_bf16 v[72:75], v[132:135], v[180:183], v[72:75]\n\t"
    "ds_read_b64_tr_b16 v[150:151], v226 offset:4864\n\t"
    "v_mfma_f32_16x16x32_bf16 v[76:79], v[128:131], v[184:187], v[76:79]\n\t"
    "v_mfma_f32_16x16x32_bf16 v[76:79], v[132:135], v[188:191], v[76:79]\n\t"
    "s_waitcnt lgkmcnt(4)\n\t"
    "v_mfma_f32_16x16x32_bf16 v[80:83], v[136:139], a[128:131], v[80:83]\n\t"
    "ds_read_b64_tr_b16 v[152:153], v227 offset:256\n\t"
    "v_mfma_f32_16x16x32_bf16 v[80:83], v[140:143], a[132:135], v[80:83]\n\t"
    "v_mfma_f32_16x16x32_bf16 v[84:87], v[136:139], a[136:139], v[84:87]\n\t"
    "v_mfma_f32_16x16x32_bf16 v[84:87], v[140:143], a[140:143], v[84:87]\n\t"
    "ds_read_b64_tr_b16 v[154:155], v227 offset:768\n\t"
    "v_mfma_f32_16x16x32_bf16 v[88:91], v[136:139], v[176:179], v[88:91]\n\t"
    "ds_read_b64_tr_b16 v[156:157], v227 offset:4352\n\t"
    "v_mfma_f32_16x16x32_bf16 v[88:91], v[140:143], v[180:183], v[88:91]\n\t"
    "ds_read_b64_tr_b16 v[158:159], v227 offset:4864\n\t"
    "v_mfma_f32_16x16x32_bf16 v[92:95], v[136:139], v[184:187], v[92:95]\n\t"
    "v_mfma_f32_16x16x32_bf16 v[92:95], v[140:143], v[188:191], v[92:95]\n\t"
    "s_waitcnt lgkmcnt(4)\n\t"
    "v_mfma_f32_16x16x32_bf16 v[96:99], v[144:147], a[128:131], v[96:99]\n\t"
    "ds_read_b64_tr_b16 v[128:129], v224 offset:32768\n\t"
    "v_mfma_f32_16x16x32_bf16 v[96:99], v[148:151], a[132:135], v[96:99]\n\t"
    "v_mfma_f32_16x16x32_bf16 v[100:103], v[144:147], a[136:139], v[100:103]\n\t"
    "v_mfma_f32_16x16x32_bf16 v[100:103], v[148:151], a[140:143], v[100:103]\n\t"
    "ds_read_b64_tr_b16 v[130:131], v224 offset:33280\n\t"
    "v_mfma_f32_16x16x32_bf16 v[104:107], v[144:147], v[176:179], v[104:107]\n\t"
    "ds_read_b64_tr_b16 v[132:133], v224 offset:36864\n\t"
    "v_mfma_f32_16x16x32_bf16 v[104:107], v[148:151], v[180:183], v[104:107]\n\t"
    "ds_read_b64_tr_b16 v[134:135], v224 offset:37376\n\t"
    "v_mfma_f32_16x16x32_bf16 v[108:111], v[144:147], v[184:187], v[108:111]\n\t"
    "v_mfma_f32_16x16x32_bf16 v[108:111], v[148:151], v[188:191], v[108:111]\n\t"
    "s_waitcnt lgkmcnt(4)\n\t"
    "v_mfma_f32_16x16x32_bf16 v[112:115], v[152:155], a[128:131], v[112:115]\n\t"
    "ds_read_b64_tr_b16 v[136:137], v225 offset:32768\n\t"
    "v_mfma_f32_16x16x32_bf16 v[112:115], v[156:159], a[132:135], v[112:115]\n\t"
    "v_mfma_f32_16x16x32_bf16 v[116:119], v[152:155], a[136:139], v[116:119]\n\t"
    "v_mfma_f32_16x16x32_bf16 v[116:119], v[156:159], a[140:143], v[116:119]\n\t"
    "ds_read_b64_tr_b16 v[138:139], v225 offset:33280\n\t"
    "v_mfma_f32_16x16x32_bf16 v[120:123], v[152:155], v[176:179], v[120:123]\n\t"
    "ds_read_b64_tr_b16 v[140:141], v225 offset:36864\n\t"
    "v_mfma_f32_16x16x32_bf16 v[120:123], v[156:159], v[180:183], v[120:123]\n\t"
    "ds_read_b64_tr_b16 v[142:143], v225 offset:37376\n\t"
    "v_mfma_f32_16x16x32_bf16 v[124:127], v[152:155], v[184:187], v[124:127]\n\t"
    "v_mfma_f32_16x16x32_bf16 v[124:127], v[156:159], v[188:191], v[124:127]\n\t"
    "s_waitcnt lgkmcnt(4)\n\t"
    "v_mfma_f32_16x16x32_bf16 v[64:67], v[128:131], a[112:115], v[64:67]\n\t"
    "ds_read_b64_tr_b16 v[144:145], v224 offset:33024\n\t"
    "v_mfma_f32_16x16x32_bf16 v[64:67], v[132:135], a[116:119], v[64:67]\n\t"
    "v_mfma_f32_16x16x32_bf16 v[68:71], v[128:131], a[120:123], v[68:71]\n\t"
    "v_mfma_f32_16x16x32_bf16 v[68:71], v[132:135], a[124:127], v[68:71]\n\t"
    "ds_read_b64_tr_b16 v[146:147], v224 offset:33536\n\t"
    "v_mfma_f32_16x16x32_bf16 v[72:75], v[128:131], v[192:195], v[72:75]\n\t"
    "ds_read_b64_tr_b16 v[148:149], v224 offset:37120\n\t"
    "v_mfma_f32_16x16x32_bf16 v[72:75], v[132:135], v[196:199], v[72:75]\n\t"
    "ds_read_b64_tr_b16 v[150:151], v224 offset:37632\n\t"
    "v_mfma_f32_16x16x32_bf16 v[76:79], v[128:131], v[200:203], v[76:79]\n\t"
    "v_mfma_f32_16x16x32_bf16 v[76:79], v[132:135], v[204:207], v[76:79]\n\t"
    "s_waitcnt lgkmcnt(4)\n\t"
    "v_mfma_f32_16x16x32_bf16 v[80:83], v[136:139], a[112:115], v[80:83]\n\t"
    "ds_read_b64_tr_b16 v[152:153], v225 offset:33024\n\t"
    "v_mfma_f32_16x16x32_bf16 v[80:83], v[140:143], a[116:119], v[80:83]\n\t"
    "v_mfma_f32_16x16x32_bf16 v[84:87], v[136:139], a[120:123], v[84:87]\n\t"
    "v_mfma_f32_16x16x32_bf16 v[84:87], v[140:143], a[124:127], v[84:87]\n\t"
    "ds_read_b64_tr_b16 v[154:155], v225 offset:33536\n\t"
    "v_mfma_f32_16x16x32_bf16 v[88:91], v[136:139], v[192:195], v[88:91]\n\t"
    "ds_read_b64_tr_b16 v[156:157], v225 offset:37120\n\t"
    "v_mfma_f32_16x16x32_bf16 v[88:91], v[140:143], v[196:199], v[88:91]\n\t"
    "ds_read_b64_tr_b16 v[158:159], v225 offset:37632\n\t"
    "v_mfma_f32_16x16x32_bf16 v[92:95], v[136:139], v[200:203], v[92:95]\n\t"
    "v_mfma_f32_16x16x32_bf16 v[92:95], v[140:143], v[204:207], v[92:95]\n\t"
    "s_waitcnt vmcnt(16)\n\t"
    "s_barrier\n\t"
    "s_waitcnt lgkmcnt(4)\n\t"
    "v_mfma_f32_16x16x32_bf16 v[96:99], v[144:147], a[112:115], v[96:99]\n\t"
    "ds_read_b64_tr_b16 v[128:129], v224 offset:49152\n\t"
    "v_mfma_f32_16x16x32_bf16 v[96:99], v[148:151], a[116:119], v[96:99]\n\t"
    "v_mfma_f32_16x16x32_bf16 v[100:103], v[144:147], a[120:123], v[100:103]\n\t"
    "v_mfma_f32_16x16x32_bf16 v[100:103], v[148:151], a[124:127], v[100:103]\n\t"
    "ds_read_b64_tr_b16 v[130:131], v224 offset:49664\n\t"
    "v_mfma_f32_16x16x32_bf16 v[104:107], v[144:147], v[192:195], v[104:107]\n\t"
    "ds_read_b64_tr_b16 v[132:133], v224 offset:53248\n\t"
    "v_mfma_f32_16x16x32_bf16 v[104:107], v[148:151], v[196:199], v[104:107]\n\t"
    "ds_read_b64_tr_b16 v[134:135], v224 offset:53760\n\t"
    "v_mfma_f32_16x16x32_bf16 v[108:111], v[144:147], v[200:203], v[108:111]\n\t"
    "v_mfma_f32_16x16x32_bf16 v[108:111], v[148:151], v[204:207], v[108:111]\n\t"
    "s_waitcnt lgkmcnt(4)\n\t"
    "v_mfma_f32_16x16x32_bf16 v[112:115], v[152:155], a[112:115], v[112:115]\n\t"
    "ds_read_b64_tr_b16 v[136:137], v225 offset:49152\n\t"
    "v_mfma_f32_16x16x32_bf16 v[112:115], v[156:159], a[116:119], v[112:115]\n\t"
    "v_mfma_f32_16x16x32_bf16 v[116:119], v[152:155], a[120:123], v[116:119]\n\t"
    "v_mfma_f32_16x16x32_bf16 v[116:119], v[156:159], a[124:127], v[116:119]\n\t"
    "ds_read_b64_tr_b16 v[138:139], v225 offset:49664\n\t"
    "v_mfma_f32_16x16x32_bf16 v[120:123], v[152:155], v[192:195], v[120:123]\n\t"
    "ds_read_b64_tr_b16 v[140:141], v225 offset:53248\n\t"
    "v_mfma_f32_16x16x32_bf16 v[120:123], v[156:159], v[196:199], v[120:123]\n\t"
    "ds_read_b64_tr_b16 v[142:143], v225 offset:53760\n\t"
    "v_mfma_f32_16x16x32_bf16 v[124:127], v[152:155], v[200:203], v[124:127]\n\t"
    "v_mfma_f32_16x16x32_bf16 v[124:127], v[156:159], v[204:207], v[124:127]\n\t"
    "s_waitcnt lgkmcnt(4)\n\t"
    "v_mfma_f32_16x16x32_bf16 v[64:67], v[128:131], a[96:99], v[64:67]\n\t"
    "ds_read_b64_tr_b16 v[144:145], v224 offset:49408\n\t"
    "v_mfma_f32_16x16x32_bf16 v[64:67], v[132:135], a[100:103], v[64:67]\n\t"
    "v_mfma_f32_16x16x32_bf16 v[68:71], v[128:131], a[104:107], v[68:71]\n\t"
    "v_mfma_f32_16x16x32_bf16 v[68:71], v[132:135], a[108:111], v[68:71]\n\t"
    "ds_read_b64_tr_b16 v[146:147], v224 offset:49920\n\t"
    "v_mfma_f32_16x16x32_bf16 v[72:75], v[128:131], v[208:211], v[72:75]\n\t"
    "ds_read_b64_tr_b16 v[148:149], v224 offset:53504\n\t"
    "v_mfma_f32_16x16x32_bf16 v[72:75], v[132:135], v[212:215], v[72:75]\n\t"
    "ds_read_b64_tr_b16 v[150:151], v224 offset:54016\n\t"
    "v_mfma_f32_16x16x32_bf16 v[76:79], v[128:131], v[216:219], v[76:79]\n\t"
    "v_mfma_f32_16x16x32_bf16 v[76:79], v[132:135], v[220:223], v[76:79]\n\t"
    "s_waitcnt lgkmcnt(4)\n\t"
    "v_mfma_f32_16x16x32_bf16 v[80:83], v[136:139], a[96:99], v[80:83]\n\t"
    "ds_read_b64_tr_b16 v[152:153], v225 offset:49408\n\t"
    "v_mfma_f32_16x16x32_bf16 v[80:83], v[140:143], a[100:103], v[80:83]\n\t"
    "v_mfma_f32_16x16x32_bf16 v[84:87], v[136:139], a[104:107], v[84:87]\n\t"
    "v_mfma_f32_16x16x32_bf16 v[84:87], v[140:143], a[108:111], v[84:87]\n\t"
    "ds_read_b64_tr_b16 v[154:155], v225 offset:49920\n\t"
    "v_mfma_f32_16x16x32_bf16 v[88:91], v[136:139], v[208:211], v[88:91]\n\t"
    "ds_read_b64_tr_b16 v[156:157], v225 offset:53504\n\t"
    "v_mfma_f32_16x16x32_bf16 v[88:91], v[140:143], v[212:215], v[88:91]\n\t"
    "ds_read_b64_tr_b16 v[158:159], v225 offset:54016\n\t"
    "v_mfma_f32_16x16x32_bf16 v[92:95], v[136:139], v[216:219], v[92:95]\n\t"
    "v_mfma_f32_16x16x32_bf16 v[92:95], v[140:143], v[220:223], v[92:95]\n\t"
    "s_waitcnt lgkmcnt(4)\n\t"
    "v_mfma_f32_16x16x32_bf16 v[96:99], v[144:147], a[96:99], v[96:99]\n\t"
    "ds_read_b64_tr_b16 v[128:129], v226 offset:32768\n\t"
    "v_mfma_f32_16x16x32_bf16 v[96:99], v[148:151], a[100:103], v[96:99]\n\t"
    "v_mfma_f32_16x16x32_bf16 v[100:103], v[144:147], a[104:107], v[100:103]\n\t"
    "v_mfma_f32_16x16x32_bf16 v[100:103], v[148:151], a[108:111], v[100:103]\n\t"
    "ds_read_b64_tr_b16 v[130:131], v226 offset:33280\n\t"
    "v_mfma_f32_16x16x32_bf16 v[104:107], v[144:147], v[208:211], v[104:107]\n\t"
    "ds_read_b64_tr_b16 v[132:133], v226 offset:36864\n\t"
    "v_mfma_f32_16x16x32_bf16 v[104:107], v[148:151], v[212:215], v[104:107]\n\t"
    "ds_read_b64_tr_b16 v[134:135], v226 offset:37376\n\t"
    "v_mfma_f32_16x16x32_bf16 v[108:111], v[144:147], v[216:219], v[108:111]\n\t"
    "v_mfma_f32_16x16x32_bf16 v[108:111], v[148:151], v[220:223], v[108:111]\n\t"
    "s_waitcnt lgkmcnt(4)\n\t"
    "v_mfma_f32_16x16x32_bf16 v[112:115], v[152:155], a[96:99], v[112:115]\n\t"
    "ds_read_b64_tr_b16 v[136:137], v227 offset:32768\n\t"
    "v_mfma_f32_16x16x32_bf16 v[112:115], v[156:159], a[100:103], v[112:115]\n\t"
    "v_mfma_f32_16x16x32_bf16 v[116:119], v[152:155], a[104:107], v[116:119]\n\t"
    "v_mfma_f32_16x16x32_bf16 v[116:119], v[156:159], a[108:111], v[116:119]\n\t"
    "ds_read_b64_tr_b16 v[138:139], v227 offset:33280\n\t"
    "v_mfma_f32_16x16x32_bf16 v[120:123], v[152:155], v[208:211], v[120:123]\n\t"
    "ds_read_b64_tr_b16 v[140:141], v227 offset:36864\n\t"
    "v_mfma_f32_16x16x32_bf16 v[120:123], v[156:159], v[212:215], v[120:123]\n\t"
    "ds_read_b64_tr_b16 v[142:143], v227 offset:37376\n\t"
    "v_mfma_f32_16x16x32_bf16 v[124:127], v[152:155], v[216:219], v[124:127]\n\t"
    "v_mfma_f32_16x16x32_bf16 v[124:127], v[156:159], v[220:223], v[124:127]\n\t"
    "s_mov_b32 exec_lo, 0\n\t"
    "s_mov_b32 exec_hi, 32\n\t"
    "buffer_load_dword v250, v248, s[52:55], 0 offen sc1\n\t"
    "s_mov_b64 exec, -1\n\t"
    "s_waitcnt vmcnt(1)\n\t"
    "s_barrier\n\t"
    "s_waitcnt lgkmcnt(4)\n\t"
    "v_mfma_f32_16x16x32_bf16 v[64:67], v[128:131], a[80:83], v[64:67]\n\t"
    "ds_read_b64_tr_b16 v[144:145], v226 offset:16384\n\t"
    "v_mfma_f32_16x16x32_bf16 v[64:67], v[132:135], a[84:87], v[64:67]\n\t"
    "v_mfma_f32_16x16x32_bf16 v[68:71], v[128:131], a[88:91], v[68:71]\n\t"
    "v_mfma_f32_16x16x32_bf16 v[68:71], v[132:135], a[92:95], v[68:71]\n\t"
    "ds_read_b64_tr_b16 v[146:147], v226 offset:16896\n\t"
    "v_mfma_f32_16x16x32_bf16 v[72:75], v[128:131], a[240:243], v[72:75]\n\t"
    "ds_read_b64_tr_b16 v[148:149], v226 offset:20480\n\t"
    "v_mfma_f32_16x16x32_bf16 v[72:75], v[132:135], a[244:247], v[72:75]\n\t"
    "ds_read_b64_tr_b16 v[150:151], v226 offset:20992\n\t"
    "v_mfma_f32_16x16x32_bf16 v[76:79], v[128:131], a[248:251], v[76:79]\n\t"
    "v_mfma_f32_16x16x32_bf16 v[76:79], v[132:135], a[252:255], v[76:79]\n\t"
    "s_waitcnt lgkmcnt(4)\n\t"
    "v_mfma_f32_16x16x32_bf16 v[80:83], v[136:139], a[80:83], v[80:83]\n\t"
    "ds_read_b64_tr_b16 v[152:153], v227 offset:16384\n\t"
    "buffer_load_dwordx4 v[0:3], v231, s[52:55], 0 offen offset:0 sc1\n\t"
    "v_mfma_f32_16x16x32_bf16 v[80:83], v[140:143], a[84:87], v[80:83]\n\t"
    "v_mfma_f32_16x16x32_bf16 v[84:87], v[136:139], a[88:91], v[84:87]\n\t"
    "buffer_load_dwordx4 v[4:7], v231, s[52:55], 0 offen offset:1024 sc1\n\t"
    "v_mfma_f32_16x16x32_bf16 v[84:87], v[140:143], a[92:95], v[84:87]\n\t"
    "ds_read_b64_tr_b16 v[154:155], v227 offset:16896\n\t"
    "v_mfma_f32_16x16x32_bf16 v[88:91], v[136:139], a[240:243], v[88:91]\n\t"
    "ds_read_b64_tr_b16 v[156:157], v227 offset:20480\n\t"
    "buffer_load_dwordx4 v[8:11], v231, s[52:55], 0 offen offset:2048 sc1\n\t"
    "ST7_%=:\n\t"
    "s_cbranch_execz ST11_%=\n\t"
    "v_mfma_f32_16x16x32_bf16 v[88:91], v[140:143], a[244:247], v[88:91]\n\t"
    "ds_read_b64_tr_b16 v[158:159], v227 offset:20992\n\t"
    "v_mfma_f32_16x16x32_bf16 v[92:95], v[136:139], a[248:251], v[92:95]\n\t"
    "buffer_load_dwordx4 v[12:15], v231, s[52:55], 0 offen offset:3072 sc1\n\t"
    "v_mfma_f32_16x16x32_bf16 v[92:95], v[140:143], a[252:255], v[92:95]\n\t"
    "s_waitcnt lgkmcnt(4)\n\t"
    "v_mfma_f32_16x16x32_bf16 v[64:67], v[144:147], a[64:67], v[64:67]\n\t"
    "ds_read_b64_tr_b16 v[128:129], v228 offset:0\n\t"
    "buffer_load_dwordx4 v[16:19], v231, s[52:55], s67 offen offset:0 sc1\n\t"
    "v_mfma_f32_16x16x32_bf16 v[64:67], v[148:151], a[68:71], v[64:67]\n\t"
    "v_mfma_f32_16x16x32_bf16 v[68:71], v[144:147], a[72:75], v[68:71]\n\t"
    "buffer_load_dwordx4 v[20:23], v231, s[52:55], s67 offen offset:1024 sc1\n\t"
    "v_mfma_f32_16x16x32_bf16 v[68:71], v[148:151], a[76:79], v[68:71]\n\t"
    "ds_read_b64_tr_b16 v[130:131], v228 offset:512\n\t"
    "v_mfma_f32_16x16x32_bf16 v[72:75], v[144:147], a[224:227], v[72:75]\n\t"
    "ds_read_b64_tr_b16 v[132:133], v228 offset:4096\n\t"
    "buffer_load_dwordx4 v[24:27], v231, s[52:55], s67 offen offset:2048 sc1\n\t"
    "v_mfma_f32_16x16x32_bf16 v[72:75], v[148:151], a[228:231], v[72:75]\n\t"
    "ds_read_b64_tr_b16 v[134:135], v228 offset:4608\n\t"
    "v_mfma_f32_16x16x32_bf16 v[76:79], v[144:147], a[232:235], v[76:79]\n\t"
    "buffer_load_dwordx4 v[28:31], v231, s[52:55], s67 offen offset:3072 sc1\n\t"
    "v_mfma_f32_16x16x32_bf16 v[76:79], v[148:151], a[236:239], v[76:79]\n\t"
    "s_waitcnt lgkmcnt(4)\n\t"
    "v_mfma_f32_16x16x32_bf16 v[80:83], v[152:155], a[64:67], v[80:83]\n\t"
    "ds_read_b64_tr_b16 v[136:137], v229 offset:0\n\t"
    "v_mfma_f32_16x16x32_bf16 v[80:83], v[156:159], a[68:71], v[80:83]\n\t"
    "v_mfma_f32_16x16x32_bf16 v[84:87], v[152:155], a[72:75], v[84:87]\n\t"
    "v_mfma_f32_16x16x32_bf16 v[84:87], v[156:159], a[76:79], v[84:87]\n\t"
    "ds_read_b64_tr_b16 v[138:139], v229 offset:512\n\t"
    "v_mfma_f32_16x16x32_bf16 v[88:91], v[152:155], a[224:227], v[88:91]\n\t"
    "ds_read_b64_tr_b16 v[140:141], v229 offset:4096\n\t"
    "v_bfe_u32 v243, v253, 4, 4\n\t"
    "v_mfma_f32_16x16x32_bf16 v[88:91], v[156:159], a[228:231], v[88:91]\n\t"
    "ds_read_b64_tr_b16 v[142:143], v229 offset:4608\n\t"
    "v_bfe_u32 v244, v253, 8, 2\n\t"
    "v_mfma_f32_16x16x32_bf16 v[92:95], v[152:155], a[232:235], v[92:95]\n\t"
    "v_mul_u32_u24 v241, 80, v243\n\t"
    "v_mfma_f32_16x16x32_bf16 v[92:95], v[156:159], a[236:239], v[92:95]\n\t"
    "v_lshl_add_u32 v241, v244, 3, v241\n\t"
    "s_waitcnt lgkmcnt(4)\n\t"
    "v_mfma_f32_16x16x32_bf16 v[64:67], v[128:131], a[48:51], v[64:67]\n\t"
    "ds_read_b64_tr_b16 v[144:145], v226 offset:33024\n\t"
    "s_and_b32 s65, s68, 1\n\t"
    "v_mfma_f32_16x16x32_bf16 v[64:67], v[132:135], a[52:55], v[64:67]\n\t"
    "s_lshl_b32 s65, s65, 13\n\t"
    "v_mfma_f32_16x16x32_bf16 v[68:71], v[128:131], a[56:59], v[68:71]\n\t"
    "s_lshr_b32 s70, s68, 1\n\t"
    "v_mfma_f32_16x16x32_bf16 v[68:71], v[132:135], a[60:63], v[68:71]\n\t"
    "ds_read_b64_tr_b16 v[146:147], v226 offset:33536\n\t"
    "s_mul_i32 s70, s70, -81920\n\t"
    "v_mfma_f32_16x16x32_bf16 v[72:75], v[128:131], a[208:211], v[72:75]\n\t"
    "ds_read_b64_tr_b16 v[148:149], v226 offset:37120\n\t"
    "s_add_u32 s65, s65, s70\n\t"
    "v_mfma_f32_16x16x32_bf16 v[72:75], v[132:135], a[212:215], v[72:75]\n\t"
    "ds_read_b64_tr_b16 v[150:151], v226 offset:37632\n\t"
    "s_add_u32 s65, s65, 147456\n\t"
    "v_mfma_f32_16x16x32_bf16 v[76:79], v[128:131], a[216:219], v[76:79]\n\t"
    "v_add_u32 v241, s65, v241\n\t"
    "v_mfma_f32_16x16x32_bf16 v[76:79], v[132:135], a[220:223], v[76:79]\n\t"
    "v_bfe_u32 v243, v253, 6, 4\n\t"
    "s_waitcnt lgkmcnt(4)\n\t"
    "v_mfma_f32_16x16x32_bf16 v[80:83], v[136:139], a[48:51], v[80:83]\n\t"
    "ds_read_b64_tr_b16 v[152:153], v227 offset:33024\n\t"
    "v_bfe_u32 v244, v253, 4, 2\n\t"
    "v_mfma_f32_16x16x32_bf16 v[80:83], v[140:143], a[52:55], v[80:83]\n\t"
    "v_mul_u32_u24 v242, 80, v243\n\t"
    "v_mfma_f32_16x16x32_bf16 v[84:87], v[136:139], a[56:59], v[84:87]\n\t"
    "v_lshl_add_u32 v242, v244, 4, v242\n\t"
    "v_mfma_f32_16x16x32_bf16 v[84:87], v[140:143], a[60:63], v[84:87]\n\t"
    "ds_read_b64_tr_b16 v[154:155], v227 offset:33536\n\t"
    "v_add_u32 v242, s65, v242\n\t"
    "v_mfma_f32_16x16x32_bf16 v[88:91], v[136:139], a[208:211], v[88:91]\n\t"
    "ds_read_b64_tr_b16 v[156:157], v227 offset:37120\n\t"
    "v_mfma_f32_16x16x32_bf16 v[88:91], v[140:143], a[212:215], v[88:91]\n\t"
    "ds_read_b64_tr_b16 v[158:159], v227 offset:37632\n\t"
    "v_mfma_f32_16x16x32_bf16 v[92:95], v[136:139], a[216:219], v[92:95]\n\t"
    "v_mfma_f32_16x16x32_bf16 v[92:95], v[140:143], a[220:223], v[92:95]\n\t"
    "s_waitcnt lgkmcnt(4)\n\t"
    "v_mfma_f32_16x16x32_bf16 v[96:99], v[144:147], a[80:83], v[96:99]\n\t"
    "ds_read_b64_tr_b16 v[128:129], v226 offset:16640\n\t"
    "v_mfma_f32_16x16x32_bf16 v[96:99], v[148:151], a[84:87], v[96:99]\n\t"
    "v_mfma_f32_16x16x32_bf16 v[100:103], v[144:147], a[88:91], v[100:103]\n\t"
    "s_mov_b32 s71, 16384\n\t"
    "s_cmp_gt_u32 s68, 1\n\t"
    "s_cselect_b32 s71, s71, 0\n\t"
    "v_and_b32 v238, 0x3fff, v253\n\t"
    "v_add_u32 v238, s71, v238\n\t"
    "v_mfma_f32_16x16x32_bf16 v[100:103], v[148:151], a[92:95], v[100:103]\n\t"
    "ds_read_b64_tr_b16 v[130:131], v226 offset:17152\n\t"
    "v_mfma_f32_16x16x32_bf16 v[104:107], v[144:147], a[240:243], v[104:107]\n\t"
    "ds_read_b64_tr_b16 v[132:133], v226 offset:20736\n\t"
    "v_xor_b32 v239, 16384, v238\n\t"
    "v_mfma_f32_16x16x32_bf16 v[104:107], v[148:151], a[244:247], v[104:107]\n\t"
    "ds_read_b64_tr_b16 v[134:135], v226 offset:21248\n\t"
    "v_mfma_f32_16x16x32_bf16 v[108:111], v[144:147], a[248:251], v[108:111]\n\t"
    "ds_write_b128 v238, v[64:67] offset:0\n\t"
    "v_mfma_f32_16x16x32_bf16 v[108:111], v[148:151], a[252:255], v[108:111]\n\t"
    "s_waitcnt lgkmcnt(5)\n\t"
    "v_mfma_f32_16x16x32_bf16 v[112:115], v[152:155], a[80:83], v[112:115]\n\t"
    "ds_read_b64_tr_b16 v[136:137], v227 offset:16640\n\t"
    "ds_write_b128 v238, v[68:71] offset:1024\n\t"
    "v_mfma_f32_16x16x32_bf16 v[112:115], v[156:159], a[84:87], v[112:115]\n\t"
    "v_mfma_f32_16x16x32_bf16 v[116:119], v[152:155], a[88:91], v[116:119]\n\t"
    "ds_write_b128 v238, v[72:75] offset:2048\n\t"
    "v_mfma_f32_16x16x32_bf16 v[116:119], v[156:159], a[92:95], v[116:119]\n\t"
    "ds_read_b64_tr_b16 v[138:139], v227 offset:17152\n\t"
    "v_mfma_f32_16x16x32_bf16 v[120:123], v[152:155], a[240:243], v[120:123]\n\t"
    "ds_read_b64_tr_b16 v[140:141], v227 offset:20736\n\t"
    "ds_write_b128 v238, v[76:79] offset:3072\n\t"
    "v_mfma_f32_16x16x32_bf16 v[120:123], v[156:159], a[244:247], v[120:123]\n\t"
    "ds_read_b64_tr_b16 v[142:143], v227 offset:21248\n\t"
    "v_mfma_f32_16x16x32_bf16 v[124:127], v[152:155], a[248:251], v[124:127]\n\t"
    "ds_write_b128 v238, v[80:83] offset:4096\n\t"
    "v_mfma_f32_16x16x32_bf16 v[124:127], v[156:159], a[252:255], v[124:127]\n\t"
    "s_waitcnt lgkmcnt(9)\n\t"
    "v_mfma_f32_16x16x32_bf16 v[96:99], v[128:131], a[64:67], v[96:99]\n\t"
    "ds_read_b64_tr_b16 v[144:145], v228 offset:256\n\t"
    "ds_write_b128 v238, v[84:87] offset:5120\n\t"
    "v_mfma_f32_16x16x32_bf16 v[96:99], v[132:135], a[68:71], v[96:99]\n\t"
    "v_mfma_f32_16x16x32_bf16 v[100:103], v[128:131], a[72:75], v[100:103]\n\t"
    "ds_write_b128 v238, v[88:91] offset:6144\n\t"
    "v_mfma_f32_16x16x32_bf16 v[100:103], v[132:135], a[76:79], v[100:103]\n\t"
    "ds_read_b64_tr_b16 v[146:147], v228 offset:768\n\t"
    "v_mfma_f32_16x16x32_bf16 v[104:107], v[128:131], a[224:227], v[104:107]\n\t"
    "ds_read_b64_tr_b16 v[148:149], v228 offset:4352\n\t"
    "ds_write_b128 v238, v[92:95] offset:7168\n\t"
    "v_mfma_f32_16x16x32_bf16 v[104:107], v[132:135], a[228:231], v[104:107]\n\t"
    "ds_read_b64_tr_b16 v[150:151], v228 offset:4864\n\t"
    "v_mfma_f32_16x16x32_bf16 v[108:111], v[128:131], a[232:235], v[108:111]\n\t"
    "v_mfma_f32_16x16x32_bf16 v[108:111], v[132:135], a[236:239], v[108:111]\n\t"
    "s_waitcnt lgkmcnt(8)\n\t"
    "v_mfma_f32_16x16x32_bf16 v[112:115], v[136:139], a[64:67], v[112:115]\n\t"
    "ds_read_b64_tr_b16 v[152:153], v229 offset:256\n\t"
    "v_mfma_f32_16x16x32_bf16 v[112:115], v[140:143], a[68:71], v[112:115]\n\t"
    "v_mfma_f32_16x16x32_bf16 v[116:119], v[136:139], a[72:75], v[116:119]\n\t"
    "v_mfma_f32_16x16x32_bf16 v[116:119], v[140:143], a[76:79], v[116:119]\n\t"
    "ds_read_b64_tr_b16 v[154:155], v229 offset:768\n\t"
    "v_mfma_f32_16x16x32_bf16 v[120:123], v[136:139], a[224:227], v[120:123]\n\t"
    "ds_read_b64_tr_b16 v[156:157], v229 offset:4352\n\t"
    "v_mfma_f32_16x16x32_bf16 v[120:123], v[140:143], a[228:231], v[120:123]\n\t"
    "ds_read_b64_tr_b16 v[158:159], v229 offset:4864\n\t"
    "v_mfma_f32_16x16x32_bf16 v[124:127], v[136:139], a[232:235], v[124:127]\n\t"
    "s_waitcnt vmcnt(0)\n\t"
    "v_max_u32 v240, v0, v1\n\t"
    "v_mfma_f32_16x16x32_bf16 v[124:127], v[140:143], a[236:239], v[124:127]\n\t"
    "v_max3_u32 v240, v2, v3, v240\n\t"
    "s_waitcnt vmcnt(9) lgkmcnt(5)\n\t"
    "s_barrier\n\t"
    "s_waitcnt lgkmcnt(4)\n\t"
    "v_mfma_f32_16x16x32_bf16 v[96:99], v[144:147], a[48:51], v[96:99]\n\t"
    "ds_read_b128 v[64:67], v239 offset:0\n\t"
    "v_max3_u32 v240, v4, v5, v240\n\t"
    "v_mfma_f32_16x16x32_bf16 v[96:99], v[148:151], a[52:55], v[96:99]\n\t"
    "ds_read_b128 v[68:71], v239 offset:1024\n\t"
    "v_max3_u32 v240, v6, v7, v240\n\t"
    "v_mfma_f32_16x16x32_bf16 v[100:103], v[144:147], a[56:59], v[100:103]\n\t"
    "ds_read_b128 v[72:75], v239 offset:2048\n\t"
    "v_max3_u32 v240, v8, v9, v240\n\t"
    "v_mfma_f32_16x16x32_bf16 v[100:103], v[148:151], a[60:63], v[100:103]\n\t"
    "ds_read_b128 v[76:79], v239 offset:3072\n\t"
    "v_max3_u32 v240, v10, v11, v240\n\t"
    "v_mfma_f32_16x16x32_bf16 v[104:107], v[144:147], a[208:211], v[104:107]\n\t"
    "ds_read_b128 v[80:83], v239 offset:4096\n\t"
    "v_max3_u32 v240, v12, v13, v240\n\t"
    "v_mfma_f32_16x16x32_bf16 v[104:107], v[148:151], a[212:215], v[104:107]\n\t"
    "ds_read_b128 v[84:87], v239 offset:5120\n\t"
    "v_max3_u32 v240, v14, v15, v240\n\t"
    "v_mfma_f32_16x16x32_bf16 v[108:111], v[144:147], a[216:219], v[108:111]\n\t"
    "ds_read_b128 v[88:91], v239 offset:6144\n\t"
    "v_max3_u32 v240, v16, v17, v240\n\t"
    "v_mfma_f32_16x16x32_bf16 v[108:111], v[148:151], a[220:223], v[108:111]\n\t"
    "ds_read_b128 v[92:95], v239 offset:7168\n\t"
    "v_max3_u32 v240, v18, v19, v240\n\t"
    "s_waitcnt lgkmcnt(8)\n\t"
    "v_mfma_f32_16x16x32_bf16 v[112:115], v[152:155], a[48:51], v[112:115]\n\t"
    "v_max3_u32 v240, v20, v21, v240\n\t"
    "v_mfma_f32_16x16x32_bf16 v[112:115], v[156:159], a[52:55], v[112:115]\n\t"
    "v_max3_u32 v240, v22, v23, v240\n\t"
    "v_mfma_f32_16x16x32_bf16 v[116:119], v[152:155], a[56:59], v[116:119]\n\t"
    "v_max3_u32 v240, v24, v25, v240\n\t"
    "v_mfma_f32_16x16x32_bf16 v[116:119], v[156:159], a[60:63], v[116:119]\n\t"
    "v_max3_u32 v240, v26, v27, v240\n\t"
    "v_mfma_f32_16x16x32_bf16 v[120:123], v[152:155], a[208:211], v[120:123]\n\t"
    "v_max3_u32 v240, v28, v29, v240\n\t"
    "v_mfma_f32_16x16x32_bf16 v[120:123], v[156:159], a[212:215], v[120:123]\n\t"
    "v_max3_u32 v240, v30, v31, v240\n\t"
    "v_mfma_f32_16x16x32_bf16 v[124:127], v[152:155], a[216:219], v[124:127]\n\t"
    "v_mfma_f32_16x16x32_bf16 v[124:127], v[156:159], a[220:223], v[124:127]\n\t"
    "v_cmp_eq_u32 vcc, -1, v240\n\t"
    "s_waitcnt lgkmcnt(0)\n\t"
    "s_nop 3\n\t"
    "s_cbranch_vccnz XS%=\n\t"
    "XR%=:\n\t"
    "v_pk_add_f32 v[0:1], v[0:1], v[64:65]\n\t"
    "v_pk_add_f32 v[2:3], v[2:3], v[66:67]\n\t"
    "v_pk_add_f32 v[4:5], v[4:5], v[68:69]\n\t"
    "v_pk_add_f32 v[6:7], v[6:7], v[70:71]\n\t"
    "v_pk_add_f32 v[8:9], v[8:9], v[72:73]\n\t"
    "v_pk_add_f32 v[10:11], v[10:11], v[74:75]\n\t"
    "v_pk_add_f32 v[12:13], v[12:13], v[76:77]\n\t"
    "v_pk_add_f32 v[14:15], v[14:15], v[78:79]\n\t"
    "v_pk_add_f32 v[16:17], v[16:17], v[80:81]\n\t"
    "v_pk_add_f32 v[18:19], v[18:19], v[82:83]\n\t"
    "v_pk_add_f32 v[20:21], v[20:21], v[84:85]\n\t"
    "v_pk_add_f32 v[22:23], v[22:23], v[86:87]\n\t"
    "v_pk_add_f32 v[24:25], v[24:25], v[88:89]\n\t"
    "v_pk_add_f32 v[26:27], v[26:27], v[90:91]\n\t"
    "v_pk_add_f32 v[28:29], v[28:29], v[92:93]\n\t"
    "v_pk_add_f32 v[30:31], v[30:31], v[94:95]\n\t"
    "v_pk_add_f32 v[0:1], v[0:1], v[96:97]\n\t"
    "v_pk_add_f32 v[2:3], v[2:3], v[98:99]\n\t"
    "v_cvt_pk_bf16_f32 v128, v0, v1\n\t"
    "v_cvt_pk_bf16_f32 v129, v2, v3\n\t"
    "ds_write_b64 v241, v[128:129] offset:0\n\t"
    "v_pk_add_f32 v[4:5], v[4:5], v[100:101]\n\t"
    "v_pk_add_f32 v[6:7], v[6:7], v[102:103]\n\t"
    "v_cvt_pk_bf16_f32 v130, v4, v5\n\t"
    "v_cvt_pk_bf16_f32 v131, v6, v7\n\t"
    "ds_write_b64 v241, v[130:131] offset:1280\n\t"
    "v_pk_add_f32 v[8:9], v[8:9], v[104:105]\n\t"
    "v_pk_add_f32 v[10:11], v[10:11], v[106:107]\n\t"
    "v_cvt_pk_bf16_f32 v132, v8, v9\n\t"
    "v_cvt_pk_bf16_f32 v133, v10, v11\n\t"
    "ds_write_b64 v241, v[132:133] offset:2560\n\t"
    "v_pk_add_f32 v[12:13], v[12:13], v[108:109]\n\t"
    "v_pk_add_f32 v[14:15], v[14:15], v[110:111]\n\t"
    "v_cvt_pk_bf16_f32 v134, v12, v13\n\t"
    "v_cvt_pk_bf16_f32 v135, v14, v15\n\t"
    "ds_write_b64 v241, v[134:135] offset:3840\n\t"
    "v_pk_add_f32 v[16:17], v[16:17], v[112:113]\n\t"
    "v_pk_add_f32 v[18:19], v[18:19], v[114:115]\n\t"
    "v_cvt_pk_bf16_f32 v136, v16, v17\n\t"
    "v_cvt_pk_bf16_f32 v137, v18, v19\n\t"
    "ds_write_b64 v241, v[136:137] offset:32\n\t"
    "v_pk_add_f32 v[20:21], v[20:21], v[116:117]\n\t"
    "v_pk_add_f32 v[22:23], v[22:23], v[118:119]\n\t"
    "v_cvt_pk_bf16_f32 v138, v20, v21\n\t"
    "v_cvt_pk_bf16_f32 v139, v22, v23\n\t"
    "ds_write_b64 v241, v[138:139] offset:1312\n\t"
    "v_pk_add_f32 v[24:25], v[24:25], v[120:121]\n\t"
    "v_pk_add_f32 v[26:27], v[26:27], v[122:123]\n\t"
    "v_cvt_pk_bf16_f32 v140, v24, v25\n\t"
    "v_cvt_pk_bf16_f32 v141, v26, v27\n\t"
    "ds_write_b64 v241, v[140:141] offset:2592\n\t"
    "v_pk_add_f32 v[28:29], v[28:29], v[124:125]\n\t"
    "v_pk_add_f32 v[30:31], v[30:31], v[126:127]\n\t"
    "v_cvt_pk_bf16_f32 v142, v28, v29\n\t"
    "v_cvt_pk_bf16_f32 v143, v30, v31\n\t"
    "ds_write_b64 v241, v[142:143] offset:3872\n\t"
    "v_add_u32 v249, 17, v249\n\t"
    "ds_read_b128 v[144:147], v242 offset:0\n\t"
    "ds_read_b128 v[148:151], v242 offset:1280\n\t"
    "ds_read_b128 v[152:155], v242 offset:2560\n\t"
    "ds_read_b128 v[156:159], v242 offset:3840\n\t"
    "s_mov_b32 s67, 32768\n\t"
    "s_mov_b32 s66, 131072\n\t"
    "s_mov_b32 s71, 163840\n\t"
    "s_waitcnt lgkmcnt(3)\n\t"
    "buffer_store_dwordx4 v[144:147], v254, s[56:59], 0 offen sc1\n\t"
    "s_waitcnt lgkmcnt(2)\n\t"
    "buffer_store_dwordx4 v[148:151], v254, s[56:59], s67 offen sc1\n\t"
    "s_waitcnt lgkmcnt(1)\n\t"
    "buffer_store_dwordx4 v[152:155], v254, s[56:59], s66 offen sc1\n\t"
    "s_waitcnt lgkmcnt(0)\n\t"
    "buffer_store_dwordx4 v[156:159], v254, s[56:59], s71 offen sc1\n\t"
    "s_cmp_lg_u32 s68, 0\n\t"
    "s_cbranch_scc1 E1%=\n\t"
    "v_cmp_eq_u32 vcc, v250, v249\n\t"
    "s_nop 0\n\t"
    "s_and_b32 s71, vcc_hi, 32\n\t"
    "s_cmp_eq_u32 s71, 32\n\t"
    "s_cbranch_scc0 SA%=\n\t"
    "RA%=:\n\t"
    "E1%=:\n\t"
    "s_endpgm\n\t"
    "HS%=:\n\t"
    "ST8_%=:\n\t"
    "s_cbranch_execz STR%=\n\t"
    "s_mov_b64 exec, 65535\n\t"
    "buffer_load_dword v250, v248, s[52:55], 0 offen sc1\n\t"
    "s_mov_b64 exec, -1\n\t"
    "s_waitcnt vmcnt(0)\n\t"
    "s_barrier\n\t"
    "s_cmp_lg_u32 s68, 0\n\t"
    "s_cbranch_scc1 F2Q%=\n\t"
    "v_add_u32 v235, 1, v235\n\t"
    "s_mov_b32 exec_lo, 0\n\t"
    "s_mov_b32 exec_hi, 8\n\t"
    "buffer_store_dword v235, v248, s[52:55], 0 offen sc1\n\t"
    "s_mov_b64 exec, -1\n\t"
    "F2Q%=:\n\t"
    "v_cmp_ne_u32 vcc, v250, v249\n\t"
    "s_nop 0\n\t"
    "s_and_b32 s71, vcc_lo, 65535\n\t"
    "s_cmp_eq_u32 s71, 65535\n\t"
    "s_cbranch_scc1 DH%=\n\t"
    "s_mov_b64 exec, 65535\n\t"
    "buffer_load_dword v250, v248, s[52:55], 0 offen sc1\n\t"
    "s_mov_b64 exec, -1\n\t"
    "s_nop 15\n\t"
    "s_nop 15\n\t"
    "WH%=:\n\t"
    "s_mov_b64 exec, 65535\n\t"
    "buffer_load_dword v234, v248, s[52:55], 0 offen sc1\n\t"
    "s_mov_b64 exec, -1\n\t"
    "s_waitcnt vmcnt(1)\n\t"
    "v_cmp_ne_u32 vcc, v250, v249\n\t"
    "s_nop 0\n\t"
    "s_and_b32 s71, vcc_lo, 65535\n\t"
    "s_cmp_eq_u32 s71, 65535\n\t"
    "s_cbranch_scc1 DH%=\n\t"
    "s_mov_b64 exec, 65535\n\t"
    "buffer_load_dword v250, v248, s[52:55], 0 offen sc1\n\t"
    "s_mov_b64 exec, -1\n\t"
    "s_waitcnt vmcnt(1)\n\t"
    "v_cmp_ne_u32 vcc, v234, v249\n\t"
    "s_nop 0\n\t"
    "s_and_b32 s71, vcc_lo, 65535\n\t"
    "s_cmp_eq_u32 s71, 65535\n\t"
    "s_cbranch_scc1 DH%=\n\t"
    "s_sub_u32 s69, s69, 1\n\t"
    "s_cbranch_scc1 GH%=\n\t"
    "s_and_b32 s71, s69, 127\n\t"
    "s_or_b32 s71, s71, s68\n\t"
    "s_cbranch_scc1 WH%=\n\t"
    "s_waitcnt vmcnt(0)\n\t"
    "v_add_u32 v235, 1, v235\n\t"
    "s_mov_b32 exec_lo, 0\n\t"
    "s_mov_b32 exec_hi, 12\n\t"
    "buffer_store_dword v235, v248, s[52:55], 0 offen sc1\n\t"
    "s_mov_b64 exec, -1\n\t"
    "s_branch WH%=\n\t"
    "GH%=:\n\t"
    "s_mov_b32 s69, 0\n\t"
    "DH%=:\n\t"
    "ST9_%=:\n\t"
    "s_cbranch_execz STR%=\n\t"
    "s_mov_b32 s67, 0\n\t"
    "buffer_load_dwordx4 a[240:243], v252, s[52:55], s67 offen offset:0 sc1\n\t"
    "buffer_load_dwordx4 a[244:247], v252, s[52:55], s67 offen offset:1024 sc1\n\t"
    "buffer_load_dwordx4 a[248:251], v230, s[52:55], s67 offen offset:0 sc1\n\t"
    "buffer_load_dwordx4 a[252:255], v230, s[52:55], s67 offen offset:1024 sc1\n\t"
    "s_mov_b32 s67, 4096\n\t"
    "buffer_load_dwordx4 a[224:227], v252, s[52:55], s67 offen offset:0 sc1\n\t"
    "buffer_load_dwordx4 a[228:231], v252, s[52:55], s67 offen offset:1024 sc1\n\t"
    "buffer_load_dwordx4 a[232:235], v230, s[52:55], s67 offen offset:0 sc1\n\t"
    "buffer_load_dwordx4 a[236:239], v230, s[52:55], s67 offen offset:1024 sc1\n\t"
    "s_mov_b32 s67, 8192\n\t"
    "buffer_load_dwordx4 a[208:211], v252, s[52:55], s67 offen offset:0 sc1\n\t"
    "buffer_load_dwordx4 a[212:215], v252, s[52:55], s67 offen offset:1024 sc1\n\t"
    "buffer_load_dwordx4 a[216:219], v230, s[52:55], s67 offen offset:0 sc1\n\t"
    "buffer_load_dwordx4 a[220:223], v230, s[52:55], s67 offen offset:1024 sc1\n\t"
    "s_mov_b32 s67, 12288\n\t"
    "buffer_load_dwordx4 a[192:195], v252, s[52:55], s67 offen offset:0 sc1\n\t"
    "buffer_load_dwordx4 a[196:199], v252, s[52:55], s67 offen offset:1024 sc1\n\t"
    "buffer_load_dwordx4 a[200:203], v230, s[52:55], s67 offen offset:0 sc1\n\t"
    "buffer_load_dwordx4 a[204:207], v230, s[52:55], s67 offen offset:1024 sc1\n\t"
    "s_branch HJ%=\n\t"
    "SI%=:\n\t"
    "ST10_%=:\n\t"
    "s_cbranch_execz STR%=\n\t"
    "s_mov_b32 exec_lo, 4294901760\n\t"
    "s_mov_b32 exec_hi, 0\n\t"
    "buffer_load_dword v250, v248, s[52:55], 0 offen sc1\n\t"
    "s_mov_b64 exec, -1\n\t"
    "s_nop 15\n\t"
    "s_nop 15\n\t"
    "WI%=:\n\t"
    "s_mov_b32 exec_lo, 4294901760\n\t"
    "s_mov_b32 exec_hi, 0\n\t"
    "buffer_load_dword v234, v248, s[52:55], 0 offen sc1\n\t"
    "s_mov_b64 exec, -1\n\t"
    "s_waitcnt vmcnt(1)\n\t"
    "v_cmp_ne_u32 vcc, v250, v249\n\t"
    "s_nop 0\n\t"
    "s_and_b32 s71, vcc_lo, 4294901760\n\t"
    "s_cmp_eq_u32 s71, 4294901760\n\t"
    "s_cbranch_scc1 DI%=\n\t"
    "s_mov_b32 exec_lo, 4294901760\n\t"
    "s_mov_b32 exec_hi, 0\n\t"
    "buffer_load_dword v250, v248, s[52:55], 0 offen sc1\n\t"
    "s_mov_b64 exec, -1\n\t"
    "s_waitcnt vmcnt(1)\n\t"
    "v_cmp_ne_u32 vcc, v234, v249\n\t"
    "s_nop 0\n\t"
    "s_and_b32 s71, vcc_lo, 4294901760\n\t"
    "s_cmp_eq_u32 s71, 4294901760\n\t"
    "s_cbranch_scc1 DI%=\n\t"
    "s_sub_u32 s69, s69, 1\n\t"
    "s_cbranch_scc1 GI%=\n\t"
    "s_and_b32 s71, s69, 127\n\t"
    "s_or_b32 s71, s71, s68\n\t"
    "s_cbranch_scc1 WI%=\n\t"
    "s_waitcnt vmcnt(0)\n\t"
    "v_add_u32 v235, 1, v235\n\t"
    "s_mov_b32 exec_lo, 0\n\t"
    "s_mov_b32 exec_hi, 12\n\t"
    "buffer_store_dword v235, v248, s[52:55], 0 offen sc1\n\t"
    "s_mov_b64 exec, -1\n\t"
    "s_branch WI%=\n\t"
    "GI%=:\n\t"
    "s_mov_b32 s69, 0\n\t"
    "DI%=:\n\t"
    "s_branch RI%=\n\t"
    "SP%=:\n\t"
    "s_mov_b32 exec_lo, 0\n\t"
    "s_mov_b32 exec_hi, 1\n\t"
    "buffer_load_dword v250, v248, s[52:55], 0 offen sc1\n\t"
    "s_mov_b64 exec, -1\n\t"
    "s_nop 15\n\t"
    "s_nop 15\n\t"
    "WP%=:\n\t"
    "s_mov_b32 exec_lo, 0\n\t"
    "s_mov_b32 exec_hi, 1\n\t"
    "buffer_load_dword v234, v248, s[52:55], 0 offen sc1\n\t"
    "s_mov_b64 exec, -1\n\t"
    "s_waitcnt vmcnt(1)\n\t"
    "v_cmp_ne_u32 vcc, v250, v249\n\t"
    "s_nop 0\n\t"
    "s_and_b32 s71, vcc_hi, 1\n\t"
    "s_cmp_eq_u32 s71, 1\n\t"
    "s_cbranch_scc1 DP%=\n\t"
    "s_mov_b32 exec_lo, 0\n\t"
    "s_mov_b32 exec_hi, 1\n\t"
    "buffer_load_dword v250, v248, s[52:55], 0 offen sc1\n\t"
    "s_mov_b64 exec, -1\n\t"
    "s_waitcnt vmcnt(1)\n\t"
    "v_cmp_ne_u32 vcc, v234, v249\n\t"
    "s_nop 0\n\t"
    "s_and_b32 s71, vcc_hi, 1\n\t"
    "s_cmp_eq_u32 s71, 1\n\t"
    "s_cbranch_scc1 DP%=\n\t"
    "s_sub_u32 s69, s69, 1\n\t"
    "s_cbranch_scc1 GP%=\n\t"
    "s_and_b32 s71, s69, 127\n\t"
    "s_or_b32 s71, s71, s68\n\t"
    "s_cbranch_scc1 WP%=\n\t"
    "s_waitcnt vmcnt(0)\n\t"
    "v_add_u32 v235, 1, v235\n\t"
    "s_mov_b32 exec_lo, 0\n\t"
    "s_mov_b32 exec_hi, 12\n\t"
    "buffer_store_dword v235, v248, s[52:55], 0 offen sc1\n\t"
    "s_mov_b64 exec, -1\n\t"
    "s_branch WP%=\n\t"
    "GP%=:\n\t"
    "s_mov_b32 s69, 0\n\t"
    "DP%=:\n\t"
    "s_branch RP%=\n\t"
    "XS%=:\n\t"
    "ST11_%=:\n\t"
    "s_cbranch_execz STR%=\n\t"
    "buffer_load_dwordx4 v[0:3], v231, s[52:55], 0 offen offset:0 sc1\n\t"
    "buffer_load_dwordx4 v[4:7], v231, s[52:55], 0 offen offset:1024 sc1\n\t"
    "buffer_load_dwordx4 v[8:11], v231, s[52:55], 0 offen offset:2048 sc1\n\t"
    "buffer_load_dwordx4 v[12:15], v231, s[52:55], 0 offen offset:3072 sc1\n\t"
    "buffer_load_dwordx4 v[16:19], v231, s[52:55], s67 offen offset:0 sc1\n\t"
    "buffer_load_dwordx4 v[20:23], v231, s[52:55], s67 offen offset:1024 sc1\n\t"
    "buffer_load_dwordx4 v[24:27], v231, s[52:55], s67 offen offset:2048 sc1\n\t"
    "buffer_load_dwordx4 v[28:31], v231, s[52:55], s67 offen offset:3072 sc1\n\t"
    "s_waitcnt vmcnt(0)\n\t"
    "v_max_u32 v240, v0, v1\n\t"
    "v_max3_u32 v240, v2, v3, v240\n\t"
    "v_max3_u32 v240, v4, v5, v240\n\t"
    "v_max3_u32 v240, v6, v7, v240\n\t"
    "v_max3_u32 v240, v8, v9, v240\n\t"
    "v_max3_u32 v240, v10, v11, v240\n\t"
    "v_max3_u32 v240, v12, v13, v240\n\t"
    "v_max3_u32 v240, v14, v15, v240\n\t"
    "v_max3_u32 v240, v16, v17, v240\n\t"
    "v_max3_u32 v240, v18, v19, v240\n\t"
    "v_max3_u32 v240, v20, v21, v240\n\t"
    "v_max3_u32 v240, v22, v23, v240\n\t"
    "v_max3_u32 v240, v24, v25, v240\n\t"
    "v_max3_u32 v240, v26, v27, v240\n\t"
    "v_max3_u32 v240, v28, v29, v240\n\t"
    "v_max3_u32 v240, v30, v31, v240\n\t"
    "v_cmp_eq_u32 vcc, -1, v240\n\t"
    "s_sub_u32 s69, s69, 1\n\t"
    "s_cbranch_scc1 XG%=\n\t"
    "s_nop 3\n\t"
    "s_cbranch_vccz XR%=\n\t"
    "s_and_b32 s71, s69, 127\n\t"
    "s_or_b32 s71, s71, s68\n\t"
    "s_cbranch_scc1 XS%=\n\t"
    "v_add_u32 v235, 1, v235\n\t"
    "s_mov_b32 exec_lo, 0\n\t"
    "s_mov_b32 exec_hi, 12\n\t"
    "buffer_store_dword v235, v248, s[52:55], 0 offen sc1\n\t"
    "s_mov_b64 exec, -1\n\t"
    "s_branch XS%=\n\t"
    "XG%=:\n\t"
    "s_mov_b32 s69, 0\n\t"
    "s_branch XR%=\n\t"
    "SA%=:\n\t"
    "s_mov_b32 exec_lo, 0\n\t"
    "s_mov_b32 exec_hi, 32\n\t"
    "buffer_load_dword v250, v248, s[52:55], 0 offen sc1\n\t"
    "s_mov_b64 exec, -1\n\t"
    "s_nop 15\n\t"
    "s_nop 15\n\t"
    "WA%=:\n\t"
    "s_mov_b32 exec_lo, 0\n\t"
    "s_mov_b32 exec_hi, 32\n\t"
    "buffer_load_dword v234, v248, s[52:55], 0 offen sc1\n\t"
    "s_mov_b64 exec, -1\n\t"
    "s_waitcnt vmcnt(1)\n\t"
    "v_cmp_eq_u32 vcc, v250, v249\n\t"
    "s_nop 0\n\t"
    "s_and_b32 s71, vcc_hi, 32\n\t"
    "s_cmp_eq_u32 s71, 32\n\t"
    "s_cbranch_scc1 DA%=\n\t"
    "s_mov_b32 exec_lo, 0\n\t"
    "s_mov_b32 exec_hi, 32\n\t"
    "buffer_load_dword v250, v248, s[52:55], 0 offen sc1\n\t"
    "s_mov_b64 exec, -1\n\t"
    "s_waitcnt vmcnt(1)\n\t"
    "v_cmp_eq_u32 vcc, v234, v249\n\t"
    "s_nop 0\n\t"
    "s_and_b32 s71, vcc_hi, 32\n\t"
    "s_cmp_eq_u32 s71, 32\n\t"
    "s_cbranch_scc1 DA%=\n\t"
    "s_sub_u32 s69, s69, 1\n\t"
    "s_cbranch_scc1 GA%=\n\t"
    "s_and_b32 s71, s69, 127\n\t"
    "s_or_b32 s71, s71, s68\n\t"
    "s_cbranch_scc1 WA%=\n\t"
    "s_waitcnt vmcnt(0)\n\t"
    "v_add_u32 v235, 1, v235\n\t"
    "s_mov_b32 exec_lo, 0\n\t"
    "s_mov_b32 exec_hi, 12\n\t"
    "buffer_store_dword v235, v248, s[52:55], 0 offen sc1\n\t"
    "s_mov_b64 exec, -1\n\t"
    "s_branch WA%=\n\t"
    "GA%=:\n\t"
    "s_mov_b32 s69, 0\n\t"
    "DA%=:\n\t"
    "s_branch RA%=\n\t"
    "CODE_END%=:\n\t"
    : "+{v0}"(tid)
    : "{s[0:1]}"(__builtin_amdgcn_kernarg_segment_ptr()), "{s2}"(blockIdx.x)
    : "memory", "scc", "vcc", "s4", "s5", "s6", "s7", "s8", "s9", "s10", "s11", "s12", "s13", "s14", "s15", "s16", "s17", "s18", "s19", "s20", "s21", "s22", "s23", "s24", "s25", "s26", "s27", "s28", "s29", "s30", "s31", "s36", "s37", "s38", "s39", "s40", "s41", "s42", "s43", "s44", "s45", "s46", "s47", "s48", "s49", "s50", "s51", "s52", "s53", "s54", "s55", "s56", "s57", "s58", "s59", "s60", "s61", "s62", "s63", "s64", "s65", "s66", "s67", "s68", "s69", "s70", "s71", "s72", "s73", "s74", "s75", "s76", "s77", "s78", "s79", "s80", "s81", "s82", "s83", "v1", "v2", "v3", "v4", "v5", "v6", "v7", "v8", "v9", "v10", "v11", "v12", "v13", "v14", "v15", "v16", "v17", "v18", "v19", "v20", "v21", "v22", "v23", "v24", "v25", "v26", "v27", "v28", "v29", "v30", "v31", "v32", "v33", "v34", "v35", "v36", "v37", "v38", "v39", "v40", "v41", "v42", "v43", "v44", "v45", "v46", "v47", "v48", "v49", "v50", "v51", "v52", "v53", "v54", "v55", "v56", "v57", "v58", "v59", "v60", "v61", "v62", "v63", "v64", "v65", "v66", "v67", "v68", "v69", "v70", "v71", "v72", "v73", "v74", "v75", "v76", "v77", "v78", "v79", "v80", "v81", "v82", "v83", "v84", "v85", "v86", "v87", "v88", "v89", "v90", "v91", "v92", "v93", "v94", "v95", "v96", "v97", "v98", "v99", "v100", "v101", "v102", "v103", "v104", "v105", "v106", "v107", "v108", "v109", "v110", "v111", "v112", "v113", "v114", "v115", "v116", "v117", "v118", "v119", "v120", "v121", "v122", "v123", "v124", "v125", "v126", "v127", "v128", "v129", "v130", "v131", "v132", "v133", "v134", "v135", "v136", "v137", "v138", "v139", "v140", "v141", "v142", "v143", "v144", "v145", "v146", "v147", "v148", "v149", "v150", "v151", "v152", "v153", "v154", "v155", "v156", "v157", "v158", "v159", "v160", "v161", "v162", "v163", "v164", "v165", "v166", "v167", "v168", "v169", "v170", "v171", "v172", "v173", "v174", "v175", "v176", "v177", "v178", "v179", "v180", "v181", "v182", "v183", "v184", "v185", "v186", "v187", "v188", "v189", "v190", "v191", "v192", "v193", "v194", "v195", "v196", "v197", "v198", "v199", "v200", "v201", "v202", "v203", "v204", "v205", "v206", "v207", "v208", "v209", "v210", "v211", "v212", "v213", "v214", "v215", "v216", "v217", "v218", "v219", "v220", "v221", "v222", "v223", "v224", "v225", "v226", "v227", "v228", "v229", "v230", "v231", "v232", "v233", "v234", "v235", "v236", "v237", "v238", "v239", "v240", "v241", "v242", "v243", "v244", "v245", "v246", "v247", "v248", "v249", "v250", "v251", "v252", "v253", "v254", "v255", "a0", "a1", "a2", "a3", "a4", "a5", "a6", "a7", "a8", "a9", "a10", "a11", "a12", "a13", "a14", "a15", "a16", "a17", "a18", "a19", "a20", "a21", "a22", "a23", "a24", "a25", "a26", "a27", "a28", "a29", "a30", "a31", "a32", "a33", "a34", "a35", "a36", "a37", "a38", "a39", "a40", "a41", "a42", "a43", "a44", "a45", "a46", "a47", "a48", "a49", "a50", "a51", "a52", "a53", "a54", "a55", "a56", "a57", "a58", "a59", "a60", "a61", "a62", "a63", "a64", "a65", "a66", "a67", "a68", "a69", "a70", "a71", "a72", "a73", "a74", "a75", "a76", "a77", "a78", "a79", "a80", "a81", "a82", "a83", "a84", "a85", "a86", "a87", "a88", "a89", "a90", "a91", "a92", "a93", "a94", "a95", "a96", "a97", "a98", "a99", "a100", "a101", "a102", "a103", "a104", "a105", "a106", "a107", "a108", "a109", "a110", "a111", "a112", "a113", "a114", "a115", "a116", "a117", "a118", "a119", "a120", "a121", "a122", "a123", "a124", "a125", "a126", "a127", "a128", "a129", "a130", "a131", "a132", "a133", "a134", "a135", "a136", "a137", "a138", "a139", "a140", "a141", "a142", "a143", "a144", "a145", "a146", "a147", "a148", "a149", "a150", "a151", "a152", "a153", "a154", "a155", "a156", "a157", "a158", "a159", "a160", "a161", "a162", "a163", "a164", "a165", "a166", "a167", "a168", "a169", "a170", "a171", "a172", "a173", "a174", "a175", "a176", "a177", "a178", "a179", "a180", "a181", "a182", "a183", "a184", "a185", "a186", "a187", "a188", "a189", "a190", "a191", "a192", "a193", "a194", "a195", "a196", "a197", "a198", "a199", "a200", "a201", "a202", "a203", "a204", "a205", "a206", "a207", "a208", "a209", "a210", "a211", "a212", "a213", "a214", "a215", "a216", "a217", "a218", "a219", "a220", "a221", "a222", "a223", "a224", "a225", "a226", "a227", "a228", "a229", "a230", "a231", "a232", "a233", "a234", "a235", "a236", "a237", "a238", "a239", "a240", "a241", "a242", "a243", "a244", "a245", "a246", "a247", "a248", "a249", "a250", "a251", "a252", "a253", "a254", "a255");
}

extern "C" void kernel_launch(void* const* d_in, const int* in_sizes, int n_in, void* d_out, int out_size, void* d_ws, size_t ws_size, hipStream_t stream) {
  (void)hipFuncSetAttribute((const void*)swiglu_kernel, hipFuncAttributeMaxDynamicSharedMemorySize, 163840);
  swiglu_kernel<<<256, 256, 163840, stream>>>(d_in[0], d_in[1], d_in[2], d_in[3], d_ws, d_out);
}
```
